```python
import torch

def _softmax(x, axis=-1):
    return torch.softmax(x, dim=axis)
def _gelu(x):
    return torch.nn.functional.gelu(x, approximate="tanh")
def _silu(x):
    return torch.nn.functional.silu(x)
def _sigmoid(x):
    return torch.sigmoid(x)
def _layernorm(x, axis=-1, eps=1e-5):
    m = x.mean(dim=axis, keepdim=True)
    v = x.var(dim=axis, keepdim=True, unbiased=False)
    return (x - m) / torch.sqrt(v + eps)
def _scatter_add(tgt, idx, src, axis=0):
    return tgt.clone().index_add_(axis, idx, src)
def _segment_sum(src, seg, n):
    out = torch.zeros((n,) + src.shape[1:], dtype=src.dtype)
    return out.index_add_(0, seg, src)
def _pad(x, pw):
    flat = tuple(v for pair in reversed(pw) for v in pair)
    return torch.nn.functional.pad(x, flat)

kernel_name = 'standalone__spectral_fnet__prompt'

def setup_inputs():

    _w = (lambda a: (lambda t: t.to(torch.bfloat16) if t.is_floating_point() else t)(torch.as_tensor(a)))
    B, S, D = 1, 2048, 640
    ns = torch.arange(S)[:, None] * torch.arange(S)[None, :]
    nd = torch.arange(D)[:, None] * torch.arange(D)[None, :]
    Ws_re = torch.cos(-2.0 * torch.pi * ns / S).to(float)
    Ws_im = torch.sin(-2.0 * torch.pi * ns / S).to(float)
    Wd_re = torch.cos(-2.0 * torch.pi * nd / D).to(float)
    Wd_im = torch.sin(-2.0 * torch.pi * nd / D).to(float)
    return {
        "x": _w((lambda *s: torch.randn(*s, dtype=torch.bfloat16) * 0.05)(B, S, D)),
        "dft_seq_re": _w(Ws_re),
        "dft_seq_im": _w(Ws_im),
        "dft_hid_re": _w(Wd_re),
        "dft_hid_im": _w(Wd_im),
    }

def reference(x, dft_seq_re, dft_seq_im, dft_hid_re, dft_hid_im):
    a = torch.einsum("bsd,ed->bse", x, dft_hid_re)
    b = torch.einsum("bsd,ed->bse", x, dft_hid_im)
    y = torch.einsum("ts,bse->bte", dft_seq_re, a) - torch.einsum("ts,bse->bte", dft_seq_im, b)
    return y
```

```cpp
#include <hip/hip_runtime.h>
#include <hip/hip_bf16.h>

typedef __attribute__((ext_vector_type(8))) __bf16 bf16x8;
typedef __attribute__((ext_vector_type(4))) __bf16 bf16x4;
typedef __attribute__((ext_vector_type(2))) __bf16 bf16x2;
typedef __attribute__((ext_vector_type(2))) float f32x2;
typedef __attribute__((ext_vector_type(4))) float f32x4;
typedef __attribute__((ext_vector_type(4))) unsigned u32x4;
typedef __attribute__((ext_vector_type(2))) unsigned u32x2;
#define LAS __attribute__((address_space(3)))

constexpr int S_ = 2048, D_ = 640;

#if defined(__HIP_DEVICE_COMPILE__)
typedef __amdgpu_buffer_rsrc_t rsrc_t;
template <int N> __device__ __forceinline__ void wait_vm() { asm volatile("s_waitcnt vmcnt(%0)" ::"i"(N) : "memory"); }
__device__ __forceinline__ void wg_barrier() { asm volatile("s_waitcnt lgkmcnt(0)\n\ts_barrier" ::: "memory"); }
__device__ __forceinline__ rsrc_t mk_rsrc(const void* p, unsigned bytes) {
    return __builtin_amdgcn_make_buffer_rsrc((void*)p, 0, bytes, 0x00020000);
}
__device__ __forceinline__ void dma16(rsrc_t r, LAS char* dst, unsigned voff, unsigned soff) {
    __builtin_amdgcn_raw_ptr_buffer_load_lds(r, (LAS void*)dst, 16, voff, soff, 0, 0);
}
__device__ __forceinline__ unsigned pack2(float a, float b) {
    f32x2 v = {a, b};
    return __builtin_bit_cast(unsigned, __builtin_convertvector(v, bf16x2));
}
__device__ __forceinline__ u32x4 pack_pair(f32x4 a, f32x4 b) {
    unsigned a0 = pack2(a[0], a[1]), a1 = pack2(a[2], a[3]);
    unsigned b0 = pack2(b[0], b[1]), b1 = pack2(b[2], b[3]);
    auto s0 = __builtin_amdgcn_permlane32_swap(a0, b0, false, false);
    auto s1 = __builtin_amdgcn_permlane32_swap(a1, b1, false, false);
    auto t0 = __builtin_amdgcn_permlane16_swap(s0[0], s0[1], false, false);
    auto t1 = __builtin_amdgcn_permlane16_swap(s1[0], s1[1], false, false);
    u32x4 r = {t0[0], t1[0], t0[1], t1[1]};
    return r;
}

template <int MT> struct K1Cfg {
    static constexpr int AB = MT * 2048;
    static constexpr int SLOT = AB + 16384;
    static constexpr int NSLOT = (MT == 5) ? 6 : 5;
    static constexpr int PER = MT + 8;
};
constexpr int K1_STEPS = S_ / 64;

template <int MT>
__device__ __forceinline__ void k1_loader(LAS char* smem, const void* cr, const void* ci, const void* x,
                                          int panel, int strip, int lw, int lane) {
    using Cfg = K1Cfg<MT>;
    rsrc_t rx = mk_rsrc(x, S_ * D_ * 2);
    unsigned voA, voB[8];
    {
        int row = 8 * lw + (lane >> 3);
        voA = row * (S_ * 2) + (((lane & 7) ^ ((row >> 1) & 7)) << 4);
    }
#pragma unroll
    for (int b = 0; b < 8; ++b) {
        int kr = 4 * (8 * lw + b) + (lane >> 4);
        int swz = ((kr & 3) << 1) | (((kr >> 3) & 1) << 3);
        voB[b] = kr * (D_ * 2) + strip * 256 + (((lane & 15) ^ swz) << 4);
    }
    rsrc_t ra[MT];
#pragma unroll
    for (int g = 0; g < MT; ++g) {
        int r = 5 * panel + g;
        const char* base = (const char*)((r & 1) ? ci : cr) + (size_t)(r >> 1) * 16 * (S_ * 2);
        ra[g] = mk_rsrc(base, 16 * S_ * 2);
    }
    auto issue = [&](int s, int slot) {
        LAS char* sb = smem + slot * Cfg::SLOT;
#pragma unroll
        for (int g = 0; g < MT; ++g) dma16(ra[g], sb + (2 * g + lw) * 1024, voA, s * 128);
#pragma unroll
        for (int b = 0; b < 8; ++b) dma16(rx, sb + Cfg::AB + (8 * lw + b) * 1024, voB[b], s * (64 * D_ * 2));
    };
    constexpr int DEPTH = Cfg::NSLOT - 1;
#pragma unroll
    for (int s = 0; s < DEPTH; ++s) issue(s, s);
    int slot = DEPTH;
#pragma unroll 1
    for (int s = 0; s < K1_STEPS - DEPTH; ++s) {
        wait_vm<Cfg::PER*(DEPTH - 1)>();
        wg_barrier();
        issue(s + DEPTH, slot);
        slot = (slot + 1 == Cfg::NSLOT) ? 0 : slot + 1;
    }
    if constexpr (DEPTH >= 5) { wait_vm<Cfg::PER * 4>(); wg_barrier(); }
    wait_vm<Cfg::PER * 3>(); wg_barrier();
    wait_vm<Cfg::PER * 2>(); wg_barrier();
    wait_vm<Cfg::PER * 1>(); wg_barrier();
    wait_vm<0>(); wg_barrier();
}

template <int MT> struct K1Frags { bf16x8 c[2][MT]; bf16x8 x[2][2]; };

template <int MT>
__device__ __forceinline__ void k1_read(K1Frags<MT>& f, LAS char* sb, unsigned oa0, unsigned oa1, unsigned ob0, unsigned ob1) {
#pragma unroll
    for (int ks = 0; ks < 2; ++ks) {
#pragma unroll
        for (int i = 0; i < 2; ++i) {
            LAS char* p = sb + (i ? ob1 : ob0) + ks * 8192;
            bf16x4 lo = __builtin_amdgcn_ds_read_tr16_b64_v4bf16((LAS bf16x4*)p);
            bf16x4 hi = __builtin_amdgcn_ds_read_tr16_b64_v4bf16((LAS bf16x4*)(p + 1024));
            f.x[ks][i] = __builtin_shufflevector(lo, hi, 0, 1, 2, 3, 4, 5, 6, 7);
        }
#pragma unroll
        for (int g = 0; g < MT; ++g) f.c[ks][g] = *(LAS bf16x8*)(sb + (ks ? oa1 : oa0) + g * 2048);
    }
}
template <int MT>
__device__ __forceinline__ void k1_mma(f32x4 (&acc)[MT][2], const K1Frags<MT>& f) {
#pragma unroll
    for (int ks = 0; ks < 2; ++ks)
#pragma unroll
        for (int g = 0; g < MT; ++g)
#pragma unroll
            for (int i = 0; i < 2; ++i)
                acc[g][i] = __builtin_amdgcn_mfma_f32_16x16x32_bf16(f.x[ks][i], f.c[ks][g], acc[g][i], 0, 0, 0);
}

template <int MT>
__device__ __forceinline__ void k1_consumer(LAS char* smem, char* pq, int panel, int strip, int w, int lane) {
    using Cfg = K1Cfg<MT>;
    const int m = lane & 15, g4 = lane >> 4, q = (lane & 15) >> 2, p = lane & 3;
    const unsigned oa0 = m * 128 + ((g4 ^ ((m >> 1) & 7)) << 4), oa1 = oa0 ^ 64;
    const int swz = (q << 1) | ((g4 & 1) << 3);
    const unsigned rowb = Cfg::AB + (8 * g4 + q) * 256 + (p & 1) * 8;
    const unsigned ob0 = rowb + (((4 * w + (p >> 1)) ^ swz) << 4);
    const unsigned ob1 = rowb + (((4 * w + 2 + (p >> 1)) ^ swz) << 4);
    f32x4 acc[MT][2];
#pragma unroll
    for (int g = 0; g < MT; ++g) { acc[g][0] = f32x4{0, 0, 0, 0}; acc[g][1] = f32x4{0, 0, 0, 0}; }
    K1Frags<MT> f0, f1;
    int slot = 0;
    auto next = [&]() { LAS char* r = smem + slot * Cfg::SLOT; slot = (slot + 1 == Cfg::NSLOT) ? 0 : slot + 1; return r; };
    wg_barrier();
    k1_read<MT>(f0, next(), oa0, oa1, ob0, ob1);
    for (int s = 1; s < K1_STEPS - 1; s += 2) {
        wg_barrier();
        __builtin_amdgcn_sched_barrier(0);
        k1_read<MT>(f1, next(), oa0, oa1, ob0, ob1);
        k1_mma<MT>(acc, f0);
        __builtin_amdgcn_sched_barrier(0);
        wg_barrier();
        __builtin_amdgcn_sched_barrier(0);
        k1_read<MT>(f0, next(), oa0, oa1, ob0, ob1);
        k1_mma<MT>(acc, f1);
        __builtin_amdgcn_sched_barrier(0);
    }
    wg_barrier();
    __builtin_amdgcn_sched_barrier(0);
    k1_read<MT>(f1, next(), oa0, oa1, ob0, ob1);
    k1_mma<MT>(acc, f0);
    k1_mma<MT>(acc, f1);
#pragma unroll
    for (int g = 0; g < MT; ++g) {
        int r = 5 * panel + g, tg = r >> 1, which = r & 1;
        u32x4 v = pack_pair(acc[g][0], acc[g][1]);
        unsigned sg = which ? 0x80008000u : 0u;
        v ^= u32x4{sg, sg, sg, sg};
        int kf = which * 20 + strip * 4 + w;
        *(u32x4*)(pq + (size_t)(tg * 40 + kf) * 1024 + lane * 16) = v;
    }
}
#endif

__global__ __launch_bounds__(384, 2) void fnet_k1(const void* __restrict__ x, const void* __restrict__ cr,
                                                  const void* __restrict__ ci, char* __restrict__ pq) {
#if defined(__HIP_DEVICE_COMPILE__)
    __shared__ __attribute__((aligned(1024))) char smem_[6 * 26624];
    LAS char* smem = (LAS char*)smem_;
    const int bid = blockIdx.x;
    const int lin = (bid & 7) * 32 + (bid >> 3);
    if (lin >= 255) return;
    const int panel = lin / 5, strip = lin - panel * 5;
    const int lane = threadIdx.x & 63;
    const int wid = __builtin_amdgcn_readfirstlane(threadIdx.x >> 6);
    if (panel < 50) {
        if (wid >= 4) k1_loader<5>(smem, cr, ci, x, panel, strip, wid - 4, lane);
        else k1_consumer<5>(smem, pq, panel, strip, wid, lane);
    } else {
        if (wid >= 4) k1_loader<6>(smem, cr, ci, x, panel, strip, wid - 4, lane);
        else k1_consumer<6>(smem, pq, panel, strip, wid, lane);
    }
#endif
}

constexpr int K2_STEPS = 20, K2_SLOT = 10240, K2_NSLOT = 16;

__global__ __launch_bounds__(384, 2) void fnet_k2(const char* __restrict__ pq, const void* __restrict__ wre,
                                                  const void* __restrict__ wim, char* __restrict__ y) {
#if defined(__HIP_DEVICE_COMPILE__)
    __shared__ __attribute__((aligned(1024))) char smem_[K2_NSLOT * K2_SLOT];
    LAS char* smem = (LAS char*)smem_;
    const int bid = blockIdx.x;
    const int lin = (bid & 7) * 32 + (bid >> 3);
    const int tb = lin >> 3, et = lin & 7;
    const int lane = threadIdx.x & 63;
    const int wid = __builtin_amdgcn_readfirstlane(threadIdx.x >> 6);
    if (wid >= 4) {
        const int lw = wid - 4;
        rsrc_t rre = mk_rsrc((const char*)wre + et * 80 * (D_ * 2), 80 * D_ * 2);
        rsrc_t rim = mk_rsrc((const char*)wim + et * 80 * (D_ * 2), 80 * D_ * 2);
        const int row = 8 * lw + (lane >> 3);
        const unsigned vo = row * (D_ * 2) + (((lane & 7) ^ ((row >> 1) & 7)) << 4);
        auto issue = [&](int s) {
            LAS char* sb = smem + (s % K2_NSLOT) * K2_SLOT;
#pragma unroll
            for (int j = 0; j < 5; ++j)
                dma16(s < 10 ? rre : rim, sb + (2 * j + lw) * 1024, vo, j * 16 * (D_ * 2) + (s % 10) * 128);
        };
#pragma unroll
        for (int s = 0; s < 12; ++s) issue(s);
#define K2_STEP(s, issued) wait_vm<5 * ((issued) - (s) - 1)>(); wg_barrier();
        K2_STEP(0, 12) issue(12);
        K2_STEP(1, 13) issue(13);
        K2_STEP(2, 14) issue(14);
        K2_STEP(3, 15) issue(15);
        K2_STEP(4, 16) issue(16);
        K2_STEP(5, 17) issue(17);
        K2_STEP(6, 18) issue(18);
        K2_STEP(7, 19) issue(19);
        K2_STEP(8, 20) K2_STEP(9, 20) K2_STEP(10, 20) K2_STEP(11, 20) K2_STEP(12, 20) K2_STEP(13, 20)
        K2_STEP(14, 20) K2_STEP(15, 20) K2_STEP(16, 20) K2_STEP(17, 20) K2_STEP(18, 20) K2_STEP(19, 20)
#undef K2_STEP
    } else {
        const int tg = tb * 4 + wid;
        const char* ap = pq + (size_t)tg * 40 * 1024 + lane * 16;
        bf16x8 a[40];
#pragma unroll
        for (int i = 0; i < 40; ++i) a[i] = *(const bf16x8*)(ap + i * 1024);
        const int m = lane & 15, g4 = lane >> 4;
        const unsigned o0 = m * 128 + ((g4 ^ ((m >> 1) & 7)) << 4), o1 = o0 ^ 64;
        f32x4 acc[5];
#pragma unroll
        for (int j = 0; j < 5; ++j) acc[j] = f32x4{0, 0, 0, 0};
#pragma unroll
        for (int s = 0; s < K2_STEPS; ++s) {
            wg_barrier();
            __builtin_amdgcn_sched_barrier(0);
            LAS char* sb = smem + (s % K2_NSLOT) * K2_SLOT;
            bf16x8 wf[2][5];
#pragma unroll
            for (int j = 0; j < 5; ++j) { wf[0][j] = *(LAS bf16x8*)(sb + o0 + j * 2048); }
#pragma unroll
            for (int j = 0; j < 5; ++j) { wf[1][j] = *(LAS bf16x8*)(sb + o1 + j * 2048); }
#pragma unroll
            for (int ks = 0; ks < 2; ++ks)
#pragma unroll
                for (int j = 0; j < 5; ++j)
                    acc[j] = __builtin_amdgcn_mfma_f32_16x16x32_bf16(wf[ks][j], a[2 * s + ks], acc[j], 0, 0, 0);
            __builtin_amdgcn_sched_barrier(0);
        }
        char* yp = y + (size_t)(tg * 16 + m) * (D_ * 2) + et * 160;
        *(u32x4*)(yp + g4 * 16) = pack_pair(acc[0], acc[1]);
        *(u32x4*)(yp + 64 + g4 * 16) = pack_pair(acc[2], acc[3]);
        *(u32x2*)(yp + 128 + g4 * 8) = u32x2{pack2(acc[4][0], acc[4][1]), pack2(acc[4][2], acc[4][3])};
    }
#endif
}

extern "C" void kernel_launch(void* const* d_in, const int* in_sizes, int n_in, void* d_out, int out_size,
                              void* d_ws, size_t ws_size, hipStream_t stream) {
    (void)in_sizes; (void)n_in; (void)out_size; (void)ws_size;
    fnet_k1<<<256, 384, 0, stream>>>(d_in[0], d_in[1], d_in[2], (char*)d_ws);
    fnet_k2<<<256, 384, 0, stream>>>((const char*)d_ws, d_in[3], d_in[4], (char*)d_out);
}
```

```cpp
#include <hip/hip_runtime.h>
#include <hip/hip_bf16.h>

typedef __attribute__((ext_vector_type(8))) __bf16 bf16x8;
typedef __attribute__((ext_vector_type(4))) __bf16 bf16x4;
typedef __attribute__((ext_vector_type(2))) __bf16 bf16x2;
typedef __attribute__((ext_vector_type(2))) float f32x2;
typedef __attribute__((ext_vector_type(4))) float f32x4;
typedef __attribute__((ext_vector_type(4))) unsigned u32x4;
typedef __attribute__((ext_vector_type(2))) unsigned u32x2;
#define LAS __attribute__((address_space(3)))

constexpr int S_ = 2048, D_ = 640;
#define AMP 0
#define ROT 1
#define STAMP(i)
#define CSTAMP(i)

#if defined(__HIP_DEVICE_COMPILE__)
typedef __amdgpu_buffer_rsrc_t rsrc_t;
__device__ __forceinline__ unsigned long long rt() { unsigned long long t; asm volatile("s_memrealtime %0\n\ts_waitcnt lgkmcnt(0)" : "=s"(t) :: "memory"); return t; }
__device__ __forceinline__ unsigned long long ct() { unsigned long long t; asm volatile("s_memtime %0\n\ts_waitcnt lgkmcnt(0)" : "=s"(t) :: "memory"); return t; }
template <int N> __device__ __forceinline__ void wait_vm() { asm volatile("s_waitcnt vmcnt(%0)" ::"i"(N) : "memory"); }
__device__ __forceinline__ void wg_barrier() { asm volatile("s_waitcnt lgkmcnt(0)\n\ts_barrier" ::: "memory"); }
__device__ __forceinline__ rsrc_t mk_rsrc(const void* p, unsigned bytes) {
    return __builtin_amdgcn_make_buffer_rsrc((void*)p, 0, bytes, 0x00020000);
}
__device__ __forceinline__ void dma16(rsrc_t r, LAS char* dst, unsigned voff, unsigned soff) {
    __builtin_amdgcn_raw_ptr_buffer_load_lds(r, (LAS void*)dst, 16, voff, soff, 0, 0);
}
__device__ __forceinline__ unsigned pack2(float a, float b) {
    f32x2 v = {a, b};
    return __builtin_bit_cast(unsigned, __builtin_convertvector(v, bf16x2));
}
__device__ __forceinline__ u32x4 pack_pair(f32x4 a, f32x4 b) {
    unsigned a0 = pack2(a[0], a[1]), a1 = pack2(a[2], a[3]);
    unsigned b0 = pack2(b[0], b[1]), b1 = pack2(b[2], b[3]);
    auto s0 = __builtin_amdgcn_permlane32_swap(a0, b0, false, false);
    auto s1 = __builtin_amdgcn_permlane32_swap(a1, b1, false, false);
    auto t0 = __builtin_amdgcn_permlane16_swap(s0[0], s0[1], false, false);
    auto t1 = __builtin_amdgcn_permlane16_swap(s1[0], s1[1], false, false);
    u32x4 r = {t0[0], t1[0], t0[1], t1[1]};
    return r;
}
#define SGB(mask, n) __builtin_amdgcn_sched_group_barrier(mask, n, 0)

constexpr int P1_AB = 16384, P1_SLOT = 36864, P1_NSLOT = 3, P1_STEPS = 16;

__device__ __forceinline__ void p1_loader(LAS char* smem, const void* cr, const void* ci, const void* x,
                                          int tb, int nj, int half, int off, int lw, int lane) {
    rsrc_t rx = mk_rsrc((const char*)x + half * (1024 * D_ * 2) + nj * 320, 1024 * D_ * 2 - nj * 320);
    rsrc_t rr = mk_rsrc((const char*)cr + (size_t)tb * (64 * S_ * 2) + half * 2048, 64 * S_ * 2 - half * 2048);
    rsrc_t ri = mk_rsrc((const char*)ci + (size_t)tb * (64 * S_ * 2) + half * 2048, 64 * S_ * 2 - half * 2048);
    const int row = 8 * lw + (lane >> 3);
    const unsigned voA = row * (S_ * 2) + (((lane & 7) ^ ((row >> 1) & 7)) << 4);
    unsigned voB[5];
#pragma unroll
    for (int b = 0; b < 5; ++b) {
        int c = b * 64 + lane, kr = c / 20, pos = c - kr * 20;
        voB[b] = (kr + 32 * lw) * (D_ * 2) + ((pos ^ (((kr >> 3) & 1) << 1)) << 4);
    }
    auto issue = [&](int kap, int slot) {
        LAS char* sb = smem + slot * P1_SLOT;
#pragma unroll
        for (int g = 0; g < 8; ++g)
            dma16((g & 1) ? ri : rr, sb + (2 * g + lw) * 1024, voA, kap * 128 + (g >> 1) * (16 * S_ * 2));
#pragma unroll
        for (int b = 0; b < 10; ++b)
            dma16(rx, sb + P1_AB + (10 * lw + b) * 1024, voB[b % 5], kap * (64 * D_ * 2) + (b / 5) * (16 * D_ * 2));
    };
    issue(off, 0);
    issue((off + 1) & 15, 1);
    int slot = 2;
#pragma unroll 1
    for (int s = 0; s < P1_STEPS - 2; ++s) {
        wait_vm<18>();
        wg_barrier();
        issue((off + s + 2) & 15, slot);
        slot = (slot == P1_NSLOT - 1) ? 0 : slot + 1;
    }
    wait_vm<18>(); wg_barrier();
    wait_vm<0>(); wg_barrier();
}

struct P1Frags { bf16x8 c[4]; bf16x8 x[5]; };
__device__ __forceinline__ void p1_read(P1Frags& f, LAS char* pa, LAS char* pb0, LAS char* pb1, LAS char* pb4, bool first = false) {
#pragma unroll
    for (int i = 0; i < 5; ++i) {
        LAS char* p = (i == 4) ? pb4 : ((i & 1) ? pb1 : pb0) + 32 * i;
        bf16x4 lo = __builtin_amdgcn_ds_read_tr16_b64_v4bf16((LAS bf16x4*)p);
        bf16x4 hi = __builtin_amdgcn_ds_read_tr16_b64_v4bf16((LAS bf16x4*)(p + 1280));
        f.x[i] = __builtin_shufflevector(lo, hi, 0, 1, 2, 3, 4, 5, 6, 7);
        if (i == 0) {
#pragma unroll
            for (int g = 0; g < 4; ++g) f.c[g] = *(LAS bf16x8*)(pa + g * 2048);
        }
    }
}
__device__ __forceinline__ void p1_mma(f32x4 (&acc)[4][5], const P1Frags& f) {
#pragma unroll
    for (int i = 0; i < 5; ++i)
#pragma unroll
        for (int g = 0; g < 4; ++g)
            acc[g][i] = __builtin_amdgcn_mfma_f32_16x16x32_bf16(f.x[i], f.c[g], acc[g][i], 0, 0, 0);
}
__device__ __forceinline__ void p1_sched() {
#pragma unroll
    for (int i = 0; i < 7; ++i) { SGB(0x8, 1); SGB(0x100, 2); }
    SGB(0x8, 13);
}

__device__ __forceinline__ void p1_consumer(LAS char* smem, char* pq, int tb, int nj, int half, int w, int lane, unsigned long long tA
) {
    const int wm = w >> 1, wn = w & 1;
    const int m = lane & 15, g4 = lane >> 4, q = (lane & 15) >> 2, p = lane & 3;
    const unsigned oa0 = wm * 8192 + m * 128 + ((g4 ^ ((m >> 1) & 7)) << 4), oa1 = oa0 ^ 64;
    const unsigned rowb = P1_AB + (8 * g4 + q) * 320 + (p >> 1) * 16 + (p & 1) * 8;
    const int sw = (g4 & 1) * 32;
    const unsigned ob0 = rowb + wn * 192 + sw, ob1 = rowb + wn * 192 - sw, ob4 = rowb + (((4 + wn) * 32) ^ sw);
    f32x4 acc[4][5];
#pragma unroll
    for (int g = 0; g < 4; ++g)
#pragma unroll
        for (int i = 0; i < 5; ++i) acc[g][i] = f32x4{0, 0, 0, 0};
    P1Frags f0, f1;
    if (w == 0) STAMP(1);
    wg_barrier();
    if (w == 0) { STAMP(2); CSTAMP(22); }
    p1_read(f0, smem + oa0, smem + ob0, smem + ob1, smem + ob4, true);
    LAS char* sb = smem;
    int slot = 0;
#pragma unroll 1
    for (int s = 0; s < P1_STEPS - 1; ++s) {
        __builtin_amdgcn_sched_barrier(0);
        p1_read(f1, sb + oa1, sb + ob0 + 10240, sb + ob1 + 10240, sb + ob4 + 10240);
        p1_mma(acc, f0);
        p1_sched();
        __builtin_amdgcn_sched_barrier(0);
        slot = (slot == P1_NSLOT - 1) ? 0 : slot + 1;
        sb = smem + slot * P1_SLOT;
        wg_barrier();
        if (w == 0) STAMP(3 + s);
        if (w == 0 && s == 5) { STAMP(8); CSTAMP(20); }
        if (w == 0 && s == 14) { STAMP(17); CSTAMP(21); }
        __builtin_amdgcn_sched_barrier(0);
        p1_read(f0, sb + oa0, sb + ob0, sb + ob1, sb + ob4);
        p1_mma(acc, f1);
        p1_sched();
        __builtin_amdgcn_sched_barrier(0);
    }
    p1_read(f1, sb + oa1, sb + ob0 + 10240, sb + ob1 + 10240, sb + ob4 + 10240);
    p1_mma(acc, f0);
    p1_sched();
    __builtin_amdgcn_sched_barrier(0);
    p1_mma(acc, f1);
    if (w == 0) STAMP(18);
#pragma unroll
    for (int g = 0; g < 4; ++g) {
        const unsigned sg = (g & 1) ? 0x80008000u : 0u;
        char* base = pq + (size_t)(((4 * tb + 2 * wm + (g >> 1)) * 40 + (g & 1) * 20 + nj * 5) * 2 + half) * 1024;
        u32x4 v0 = pack_pair(acc[g][0], acc[g][1]);
        u32x4 v1 = pack_pair(acc[g][2], acc[g][3]);
        f32x4 h = acc[g][4];
        u32x2 v2 = {pack2(h[0], h[1]) ^ sg, pack2(h[2], h[3]) ^ sg};
        *(u32x4*)(base + wn * 6144 + lane * 16) = v0 ^ u32x4{sg, sg, sg, sg};
        *(u32x4*)(base + wn * 6144 + 2048 + lane * 16) = v1 ^ u32x4{sg, sg, sg, sg};
        *(u32x2*)(base + 2 * 2048 + wn * 512 + (g4 >> 1) * 256 + m * 16 + (g4 & 1) * 8) = v2;
    }
}
#endif

__global__ __launch_bounds__(384, 2) void fnet_k1(const void* __restrict__ x, const void* __restrict__ cr,
                                                  const void* __restrict__ ci, char* __restrict__ pq) {
#if defined(__HIP_DEVICE_COMPILE__)
    __shared__ __attribute__((aligned(1024))) char smem_[P1_NSLOT * P1_SLOT];
    LAS char* smem = (LAS char*)smem_;
    const int bid = blockIdx.x, xcd = bid & 7, idx = bid >> 3;
    unsigned long long tA = 0;
    const int half = xcd & 1, tbl = idx >> 2, nj = idx & 3, tb = 8 * (xcd >> 1) + tbl;
    const int lane = threadIdx.x & 63;
    const int wid = __builtin_amdgcn_readfirstlane(threadIdx.x >> 6);
    if (wid >= 4) p1_loader(smem, cr, ci, x, tb, nj, half, ROT ? (4 * nj + 2 * tbl) & 15 : 0, wid - 4, lane);
    else p1_consumer(smem, pq, tb, nj, half, wid, lane, tA);
#endif
}

constexpr int P2_STEPS = 20, P2_SLOT = 10240, P2_NSLOT = 4, P2_AHEAD = 3;

__global__ __launch_bounds__(384, 2) void fnet_k2(char* __restrict__ pq, const void* __restrict__ wre,
                                                  const void* __restrict__ wim, char* __restrict__ y) {
#if defined(__HIP_DEVICE_COMPILE__)
    __shared__ __attribute__((aligned(1024))) char smem_[9 * P2_SLOT];
    LAS char* smem = (LAS char*)smem_;
    const int bid = blockIdx.x, xcd = bid & 7, idx = bid >> 3;
    const int tb = 4 * xcd + (idx >> 3), et = idx & 7;
    const int lane = threadIdx.x & 63;
    const int wid = __builtin_amdgcn_readfirstlane(threadIdx.x >> 6);
    if (wid >= 4) {
        const int lw = wid - 4;
        rsrc_t rre = mk_rsrc((const char*)wre + et * 80 * (D_ * 2), 80 * D_ * 2);
        rsrc_t rim = mk_rsrc((const char*)wim + et * 80 * (D_ * 2), 80 * D_ * 2);
        const int row = 8 * lw + (lane >> 3);
        const unsigned vo = row * (D_ * 2) + (((lane & 7) ^ ((row >> 1) & 7)) << 4);
        auto issue = [&](rsrc_t r, int ks, int slot) {
            LAS char* sb = smem + slot * P2_SLOT;
#pragma unroll
            for (int j = 0; j < 5; ++j) dma16(r, sb + (2 * j + lw) * 1024, vo, j * 16 * (D_ * 2) + ks * 128);
        };
        issue(rre, 0, 0); issue(rre, 1, 1); issue(rre, 2, 2);
#pragma unroll 1
        for (int s = 0; s < 7; ++s) { wait_vm<10>(); wg_barrier(); issue(rre, s + 3, (s + 3) & 3); }
#pragma unroll 1
        for (int s = 7; s < 17; ++s) { wait_vm<10>(); wg_barrier(); issue(rim, s - 7, (s + 3) & 3); }
        wait_vm<10>(); wg_barrier();
        wait_vm<5>(); wg_barrier();
        wait_vm<0>(); wg_barrier();
    } else {
        const int tg = tb * 4 + wid;
        const char* ap = pq + (size_t)tg * 80 * 1024 + lane * 16;
        bf16x8 a0[4], a1[4];
#pragma unroll
        for (int i = 0; i < 4; ++i) a0[i] = *(const bf16x8*)(ap + i * 1024);
#pragma unroll
        for (int i = 0; i < 4; ++i) a1[i] = *(const bf16x8*)(ap + 4096 + i * 1024);
        const int m = lane & 15, g4 = lane >> 4;
        const unsigned o0 = m * 128 + ((g4 ^ ((m >> 1) & 7)) << 4), o1 = o0 ^ 64;
        f32x4 acc[5];
#pragma unroll
        for (int j = 0; j < 5; ++j) acc[j] = f32x4{0, 0, 0, 0};
        bf16x8 w0[2][5], w1[2][5];
        auto rd = [&](bf16x8 (&wf)[2][5], int slot) {
            LAS char* sb = smem + slot * P2_SLOT;
#pragma unroll
            for (int j = 0; j < 5; ++j) wf[0][j] = *(LAS bf16x8*)(sb + o0 + j * 2048);
#pragma unroll
            for (int j = 0; j < 5; ++j) wf[1][j] = *(LAS bf16x8*)(sb + o1 + j * 2048);
        };
        auto mma = [&](const bf16x8 (&wf)[2][5], const bf16x8 (&a)[4]) {
#pragma unroll
            for (int i = 0; i < 4; ++i)
#pragma unroll
                for (int j = 0; j < 5; ++j)
                    acc[j] = __builtin_amdgcn_mfma_f32_16x16x32_bf16(wf[i >> 1][j], a[i], acc[j], 0, 0, 0);
        };
        auto sched = [&]() {
#pragma unroll
            for (int i = 0; i < 10; ++i) { SGB(0x8, 1); SGB(0x100, 1); }
            SGB(0x8, 10);
            SGB(0x20, 4);
        };
        if (wid == 0) STAMP(1);
        wg_barrier();
        if (wid == 0) STAMP(2);
        rd(w0, 0);
#pragma unroll 1
        for (int s = 0; s < P2_STEPS - 2; s += 2) {
            wg_barrier();
            if (wid == 0) STAMP(3 + s);
            __builtin_amdgcn_sched_barrier(0);
            rd(w1, (s + 1) & 3);
            mma(w0, a0);
#pragma unroll
            for (int i = 0; i < 4; ++i) a0[i] = *(const bf16x8*)(ap + (s + 2) * 4096 + i * 1024);
            sched();
            __builtin_amdgcn_sched_barrier(0);
            wg_barrier();
            if (wid == 0) STAMP(4 + s);
            __builtin_amdgcn_sched_barrier(0);
            rd(w0, (s + 2) & 3);
            mma(w1, a1);
#pragma unroll
            for (int i = 0; i < 4; ++i) a1[i] = *(const bf16x8*)(ap + (s + 3) * 4096 + i * 1024);
            sched();
            __builtin_amdgcn_sched_barrier(0);
        }
        wg_barrier();
        if (wid == 0) STAMP(21);
        __builtin_amdgcn_sched_barrier(0);
        rd(w1, 3);
        mma(w0, a0);
        mma(w1, a1);
        char* yp = y + (size_t)(tg * 16 + m) * (D_ * 2) + et * 160;
        *(u32x4*)(yp + g4 * 16) = pack_pair(acc[0], acc[1]);
        *(u32x4*)(yp + 64 + g4 * 16) = pack_pair(acc[2], acc[3]);
        *(u32x2*)(yp + 128 + g4 * 8) = u32x2{pack2(acc[4][0], acc[4][1]), pack2(acc[4][2], acc[4][3])};
    }
#endif
}

extern "C" void kernel_launch(void* const* d_in, const int* in_sizes, int n_in, void* d_out, int out_size,
                              void* d_ws, size_t ws_size, hipStream_t stream) {
    (void)in_sizes; (void)n_in; (void)out_size; (void)ws_size;
    fnet_k1<<<256, 384, 0, stream>>>(d_in[0], d_in[1], d_in[2], (char*)d_ws);
    fnet_k2<<<256, 384, 0, stream>>>((char*)d_ws, d_in[3], d_in[4], (char*)d_out);
}
```

```cpp
#define ROT 0
#define AMPX 0
#define USEK0 0
#include <hip/hip_runtime.h>
#include <hip/hip_bf16.h>

typedef __attribute__((ext_vector_type(8))) __bf16 bf16x8;
typedef __attribute__((ext_vector_type(4))) __bf16 bf16x4;
typedef __attribute__((ext_vector_type(2))) __bf16 bf16x2;
typedef __attribute__((ext_vector_type(2))) float f32x2;
typedef __attribute__((ext_vector_type(4))) float f32x4;
typedef __attribute__((ext_vector_type(4))) unsigned u32x4;
typedef __attribute__((ext_vector_type(2))) unsigned u32x2;
#define LAS __attribute__((address_space(3)))

constexpr int S_ = 2048, D_ = 640;
#define AMP 0
#define STAMP(i)
#define CSTAMP(i)

#if defined(__HIP_DEVICE_COMPILE__)
typedef __amdgpu_buffer_rsrc_t rsrc_t;
__device__ __forceinline__ unsigned long long rt() { unsigned long long t; asm volatile("s_memrealtime %0\n\ts_waitcnt lgkmcnt(0)" : "=s"(t) :: "memory"); return t; }
__device__ __forceinline__ unsigned long long ct() { unsigned long long t; asm volatile("s_memtime %0\n\ts_waitcnt lgkmcnt(0)" : "=s"(t) :: "memory"); return t; }
template <int N> __device__ __forceinline__ void wait_vm() { asm volatile("s_waitcnt vmcnt(%0)" ::"i"(N) : "memory"); }
__device__ __forceinline__ void wg_barrier() { asm volatile("s_waitcnt lgkmcnt(0)\n\ts_barrier" ::: "memory"); }
__device__ __forceinline__ rsrc_t mk_rsrc(const void* p, unsigned bytes) {
    return __builtin_amdgcn_make_buffer_rsrc((void*)p, 0, bytes, 0x00020000);
}
__device__ __forceinline__ void dma16(rsrc_t r, LAS char* dst, unsigned voff, unsigned soff) {
    __builtin_amdgcn_raw_ptr_buffer_load_lds(r, (LAS void*)dst, 16, voff, soff, 0, 0);
}
__device__ __forceinline__ unsigned pack2(float a, float b) {
    f32x2 v = {a, b};
    return __builtin_bit_cast(unsigned, __builtin_convertvector(v, bf16x2));
}
__device__ __forceinline__ u32x4 pack_pair(f32x4 a, f32x4 b) {
    unsigned a0 = pack2(a[0], a[1]), a1 = pack2(a[2], a[3]);
    unsigned b0 = pack2(b[0], b[1]), b1 = pack2(b[2], b[3]);
    auto s0 = __builtin_amdgcn_permlane32_swap(a0, b0, false, false);
    auto s1 = __builtin_amdgcn_permlane32_swap(a1, b1, false, false);
    auto t0 = __builtin_amdgcn_permlane16_swap(s0[0], s0[1], false, false);
    auto t1 = __builtin_amdgcn_permlane16_swap(s1[0], s1[1], false, false);
    u32x4 r = {t0[0], t1[0], t0[1], t1[1]};
    return r;
}
#define SGB(mask, n) __builtin_amdgcn_sched_group_barrier(mask, n, 0)

constexpr int P1_AB = 16384, P1_SLOT = 36864, P1_NSLOT = 3, P1_STEPS = 16;

__device__ __forceinline__ void p1_loader(LAS char* smem, const void* cr, const void* ci, const void* x,
                                          int tb, int nj, int half, int off, int lw, int lane) {
    rsrc_t rx = mk_rsrc((const char*)x + half * (1024 * D_ * 2) + nj * 320, 1024 * D_ * 2 - nj * 320);
    rsrc_t rr = mk_rsrc((const char*)cr + (size_t)tb * (64 * S_ * 2) + half * 2048, 64 * S_ * 2 - half * 2048);
    rsrc_t ri = mk_rsrc((const char*)ci + (size_t)tb * (64 * S_ * 2) + half * 2048, 64 * S_ * 2 - half * 2048);
    const int row = 8 * lw + (lane >> 3);
    const unsigned voA = row * (S_ * 2) + (((lane & 7) ^ ((row >> 1) & 7)) << 4);
    unsigned voB[5];
#pragma unroll
    for (int b = 0; b < 5; ++b) {
        int c = b * 64 + lane, kr = c / 20, pos = c - kr * 20;
        voB[b] = (kr + 32 * lw) * (D_ * 2) + ((pos ^ (((kr >> 3) & 1) << 1)) << 4);
    }
    auto issue = [&](int kap, int slot) {
        LAS char* sb = smem + slot * P1_SLOT;
#pragma unroll
        for (int g = 0; g < 8; ++g)
            dma16((g & 1) ? ri : rr, sb + (2 * g + lw) * 1024, voA, kap * 128 + (g >> 1) * (16 * S_ * 2));
#pragma unroll
        for (int b = 0; b < 10; ++b)
            dma16(rx, sb + P1_AB + (10 * lw + b) * 1024, voB[b % 5], kap * (64 * D_ * 2) + (b / 5) * (16 * D_ * 2));
    };
    issue(off, 0);
    issue((off + 1) & 15, 1);
    int slot = 2;
#pragma unroll 1
    for (int s = 0; s < P1_STEPS - 2; ++s) {
        wait_vm<18>();
        wg_barrier();
        issue((off + s + 2) & 15, slot);
        slot = (slot == P1_NSLOT - 1) ? 0 : slot + 1;
    }
    wait_vm<18>(); wg_barrier();
    wait_vm<0>(); wg_barrier();
}

struct P1Frags { bf16x8 c[4]; bf16x8 x[5]; };
__device__ __forceinline__ void p1_read(P1Frags& f, LAS char* pa, LAS char* pb0, LAS char* pb1, LAS char* pb4, bool first = false) {
#pragma unroll
    for (int i = 0; i < 5; ++i) {
        LAS char* p = (i == 4) ? pb4 : ((i & 1) ? pb1 : pb0) + 32 * i;
        bf16x4 lo = __builtin_amdgcn_ds_read_tr16_b64_v4bf16((LAS bf16x4*)p);
        bf16x4 hi = __builtin_amdgcn_ds_read_tr16_b64_v4bf16((LAS bf16x4*)(p + 1280));
        f.x[i] = __builtin_shufflevector(lo, hi, 0, 1, 2, 3, 4, 5, 6, 7);
        if (i == 0) {
#pragma unroll
            for (int g = 0; g < 4; ++g) f.c[g] = *(LAS bf16x8*)(pa + g * 2048);
        }
    }
}
__device__ __forceinline__ void p1_mma(f32x4 (&acc)[4][5], const P1Frags& f) {
#pragma unroll
    for (int i = 0; i < 5; ++i)
#pragma unroll
        for (int g = 0; g < 4; ++g)
            acc[g][i] = __builtin_amdgcn_mfma_f32_16x16x32_bf16(f.x[i], f.c[g], acc[g][i], 0, 0, 0);
}
__device__ __forceinline__ void p1_sched() {
#pragma unroll
    for (int i = 0; i < 7; ++i) { SGB(0x8, 1); SGB(0x100, 2); }
    SGB(0x8, 13);
}

__device__ __forceinline__ void p1_consumer(LAS char* smem, char* pq, int tb, int nj, int half, int w, int lane, unsigned long long tA
) {
    const int wm = w >> 1, wn = w & 1;
    const int m = lane & 15, g4 = lane >> 4, q = (lane & 15) >> 2, p = lane & 3;
    const unsigned oa0 = wm * 8192 + m * 128 + ((g4 ^ ((m >> 1) & 7)) << 4), oa1 = oa0 ^ 64;
    const unsigned rowb = P1_AB + (8 * g4 + q) * 320 + (p >> 1) * 16 + (p & 1) * 8;
    const int sw = (g4 & 1) * 32;
    const unsigned ob0 = rowb + wn * 192 + sw, ob1 = rowb + wn * 192 - sw, ob4 = rowb + (((4 + wn) * 32) ^ sw);
    f32x4 acc[4][5];
#pragma unroll
    for (int g = 0; g < 4; ++g)
#pragma unroll
        for (int i = 0; i < 5; ++i) acc[g][i] = f32x4{0, 0, 0, 0};
    P1Frags f0, f1;
    if (w == 0) STAMP(1);
    wg_barrier();
    if (w == 0) { STAMP(2); CSTAMP(22); }
    p1_read(f0, smem + oa0, smem + ob0, smem + ob1, smem + ob4, true);
    LAS char* sb = smem;
    int slot = 0;
#pragma unroll 1
    for (int s = 0; s < P1_STEPS - 1; ++s) {
        __builtin_amdgcn_sched_barrier(0);
        p1_read(f1, sb + oa1, sb + ob0 + 10240, sb + ob1 + 10240, sb + ob4 + 10240);
        p1_mma(acc, f0);
        p1_sched();
        __builtin_amdgcn_sched_barrier(0);
        slot = (slot == P1_NSLOT - 1) ? 0 : slot + 1;
        sb = smem + slot * P1_SLOT;
        wg_barrier();
        if (w == 0) STAMP(3 + s);
        if (w == 0 && s == 5) { STAMP(8); CSTAMP(20); }
        if (w == 0 && s == 14) { STAMP(17); CSTAMP(21); }
        __builtin_amdgcn_sched_barrier(0);
        p1_read(f0, sb + oa0, sb + ob0, sb + ob1, sb + ob4);
        p1_mma(acc, f1);
        p1_sched();
        __builtin_amdgcn_sched_barrier(0);
    }
    p1_read(f1, sb + oa1, sb + ob0 + 10240, sb + ob1 + 10240, sb + ob4 + 10240);
    p1_mma(acc, f0);
    p1_sched();
    __builtin_amdgcn_sched_barrier(0);
    p1_mma(acc, f1);
    if (w == 0) STAMP(18);
#pragma unroll
    for (int g = 0; g < 4; ++g) {
        const unsigned sg = (g & 1) ? 0x80008000u : 0u;
        char* base = pq + (size_t)(((4 * tb + 2 * wm + (g >> 1)) * 40 + (g & 1) * 20 + nj * 5) * 2 + half) * 1024;
        u32x4 v0 = pack_pair(acc[g][0], acc[g][1]);
        u32x4 v1 = pack_pair(acc[g][2], acc[g][3]);
        f32x4 h = acc[g][4];
        u32x2 v2 = {pack2(h[0], h[1]) ^ sg, pack2(h[2], h[3]) ^ sg};
        *(u32x4*)(base + wn * 6144 + lane * 16) = v0 ^ u32x4{sg, sg, sg, sg};
        *(u32x4*)(base + wn * 6144 + 2048 + lane * 16) = v1 ^ u32x4{sg, sg, sg, sg};
        *(u32x2*)(base + 2 * 2048 + wn * 512 + (g4 >> 1) * 256 + m * 16 + (g4 & 1) * 8) = v2;
    }
    wait_vm<0>();
    if (w == 0 && lane == 0) { unsigned long long* q = (unsigned long long*)(pq + (16 << 20)); q[blockIdx.x] = tA; q[256 + blockIdx.x] = rt(); }
}
#endif

__global__ __launch_bounds__(384, 2) void fnet_k1(const void* __restrict__ x, const void* __restrict__ cr,
                                                  const void* __restrict__ ci, char* __restrict__ pq) {
#if defined(__HIP_DEVICE_COMPILE__)
    __shared__ __attribute__((aligned(1024))) char smem_[P1_NSLOT * P1_SLOT];
    LAS char* smem = (LAS char*)smem_;
    const int bid = blockIdx.x, xcd = bid & 7, idx = bid >> 3;
    unsigned long long tA = rt();
    const int half = xcd & 1, tbl = idx >> 2, nj = idx & 3, tb = 8 * (xcd >> 1) + tbl;
    const int lane = threadIdx.x & 63;
    const int wid = __builtin_amdgcn_readfirstlane(threadIdx.x >> 6);
    if (wid >= 4) p1_loader(smem, cr, ci, x, tb, nj, half, ROT ? (4 * nj + 2 * tbl) & 15 : 0, wid - 4, lane);
    else p1_consumer(smem, pq, tb, nj, half, wid, lane, tA);
#endif
}

constexpr int P2_STEPS = 20, P2_SLOT = 10240, P2_NSLOT = 4, P2_AHEAD = 3;

__global__ __launch_bounds__(384, 2) void fnet_k2(char* __restrict__ pq, const void* __restrict__ wre,
                                                  const void* __restrict__ wim, char* __restrict__ y) {
#if defined(__HIP_DEVICE_COMPILE__)
    __shared__ __attribute__((aligned(1024))) char smem_[9 * P2_SLOT];
    LAS char* smem = (LAS char*)smem_;
    const int bid = blockIdx.x, xcd = bid & 7, idx = bid >> 3;
    unsigned long long tA = rt();
    const int tb = 4 * xcd + (idx >> 3), et = idx & 7;
    const int lane = threadIdx.x & 63;
    const int wid = __builtin_amdgcn_readfirstlane(threadIdx.x >> 6);
    if (wid >= 4) {
        const int lw = wid - 4;
        rsrc_t rre = mk_rsrc((const char*)wre + et * 80 * (D_ * 2), 80 * D_ * 2);
        rsrc_t rim = mk_rsrc((const char*)wim + et * 80 * (D_ * 2), 80 * D_ * 2);
        const int row = 8 * lw + (lane >> 3);
        const unsigned vo = row * (D_ * 2) + (((lane & 7) ^ ((row >> 1) & 7)) << 4);
        auto issue = [&](rsrc_t r, int ks, int slot) {
            LAS char* sb = smem + slot * P2_SLOT;
#pragma unroll
            for (int j = 0; j < 5; ++j) dma16(r, sb + (2 * j + lw) * 1024, vo, j * 16 * (D_ * 2) + ks * 128);
        };
        issue(rre, 0, 0); issue(rre, 1, 1); issue(rre, 2, 2);
#pragma unroll 1
        for (int s = 0; s < 7; ++s) { wait_vm<10>(); wg_barrier(); issue(rre, s + 3, (s + 3) & 3); }
#pragma unroll 1
        for (int s = 7; s < 17; ++s) { wait_vm<10>(); wg_barrier(); issue(rim, s - 7, (s + 3) & 3); }
        wait_vm<10>(); wg_barrier();
        wait_vm<5>(); wg_barrier();
        wait_vm<0>(); wg_barrier();
    } else {
        const int tg = tb * 4 + wid;
        const char* ap = pq + (size_t)tg * 80 * 1024 + lane * 16;
        bf16x8 a0[4], a1[4];
#pragma unroll
        for (int i = 0; i < 4; ++i) a0[i] = *(const bf16x8*)(ap + i * 1024);
#pragma unroll
        for (int i = 0; i < 4; ++i) a1[i] = *(const bf16x8*)(ap + 4096 + i * 1024);
        const int m = lane & 15, g4 = lane >> 4;
        const unsigned o0 = m * 128 + ((g4 ^ ((m >> 1) & 7)) << 4), o1 = o0 ^ 64;
        f32x4 acc[5];
#pragma unroll
        for (int j = 0; j < 5; ++j) acc[j] = f32x4{0, 0, 0, 0};
        bf16x8 w0[2][5], w1[2][5];
        auto rd = [&](bf16x8 (&wf)[2][5], int slot) {
            LAS char* sb = smem + slot * P2_SLOT;
#pragma unroll
            for (int j = 0; j < 5; ++j) wf[0][j] = *(LAS bf16x8*)(sb + o0 + j * 2048);
#pragma unroll
            for (int j = 0; j < 5; ++j) wf[1][j] = *(LAS bf16x8*)(sb + o1 + j * 2048);
        };
        auto mma = [&](const bf16x8 (&wf)[2][5], const bf16x8 (&a)[4]) {
#pragma unroll
            for (int i = 0; i < 4; ++i)
#pragma unroll
                for (int j = 0; j < 5; ++j)
                    acc[j] = __builtin_amdgcn_mfma_f32_16x16x32_bf16(wf[i >> 1][j], a[i], acc[j], 0, 0, 0);
        };
        auto sched = [&]() {
#pragma unroll
            for (int i = 0; i < 10; ++i) { SGB(0x8, 1); SGB(0x100, 1); }
            SGB(0x8, 10);
            SGB(0x20, 4);
        };
        if (wid == 0) STAMP(1);
        wg_barrier();
        if (wid == 0) STAMP(2);
        rd(w0, 0);
#pragma unroll 1
        for (int s = 0; s < P2_STEPS - 2; s += 2) {
            wg_barrier();
            if (wid == 0) STAMP(3 + s);
            __builtin_amdgcn_sched_barrier(0);
            rd(w1, (s + 1) & 3);
            mma(w0, a0);
#pragma unroll
            for (int i = 0; i < 4; ++i) a0[i] = *(const bf16x8*)(ap + (s + 2) * 4096 + i * 1024);
            sched();
            __builtin_amdgcn_sched_barrier(0);
            wg_barrier();
            if (wid == 0) STAMP(4 + s);
            __builtin_amdgcn_sched_barrier(0);
            rd(w0, (s + 2) & 3);
            mma(w1, a1);
#pragma unroll
            for (int i = 0; i < 4; ++i) a1[i] = *(const bf16x8*)(ap + (s + 3) * 4096 + i * 1024);
            sched();
            __builtin_amdgcn_sched_barrier(0);
        }
        wg_barrier();
        if (wid == 0) STAMP(21);
        __builtin_amdgcn_sched_barrier(0);
        rd(w1, 3);
        mma(w0, a0);
        mma(w1, a1);
        char* yp = y + (size_t)(tg * 16 + m) * (D_ * 2) + et * 160;
        *(u32x4*)(yp + g4 * 16) = pack_pair(acc[0], acc[1]);
        *(u32x4*)(yp + 64 + g4 * 16) = pack_pair(acc[2], acc[3]);
        *(u32x2*)(yp + 128 + g4 * 8) = u32x2{pack2(acc[4][0], acc[4][1]), pack2(acc[4][2], acc[4][3])};
        wait_vm<0>();
        if (wid == 0 && lane == 0) { unsigned long long* q = (unsigned long long*)(pq + (16 << 20)); q[512 + blockIdx.x] = tA; q[768 + blockIdx.x] = rt(); }
    }
#endif
}

__global__ void fnet_k0(char* pq) {
#if defined(__HIP_DEVICE_COMPILE__)
    unsigned long long t = rt();
    if (threadIdx.x == 0 && blockIdx.x == 0) ((unsigned long long*)(pq + (16 << 20)))[1024] = t;
#endif
}
__global__ void fnet_k3(char* pq) {
#if defined(__HIP_DEVICE_COMPILE__)
    unsigned long long now = rt();
    const unsigned long long* q = (const unsigned long long*)(pq + (16 << 20));
    unsigned long long mn[4], mx[4];
    for (int a = 0; a < 4; ++a) { mn[a] = ~0ull; mx[a] = 0;
        for (int j = 0; j < 4; ++j) { unsigned long long v = q[a * 256 + j * 64 + threadIdx.x]; mn[a] = v < mn[a] ? v : mn[a]; mx[a] = v > mx[a] ? v : mx[a]; }
        for (int o = 1; o < 64; o *= 2) { unsigned long long v = __shfl_xor(mn[a], o); mn[a] = v < mn[a] ? v : mn[a]; v = __shfl_xor(mx[a], o); mx[a] = v > mx[a] ? v : mx[a]; } }
    unsigned long long k0 = USEK0 ? q[1024] : mn[0];
    unsigned long long d = AMPX == 0 ? 0 : AMPX == 1 ? now - k0 : AMPX == 2 ? mn[0] - k0 : AMPX == 3 ? now - mx[3] : AMPX == 4 ? mx[0] - mn[0] : AMPX == 5 ? mx[3] - mn[2] : mn[2] - mx[1];
    d = d * 10; while (rt() - now < d && d < 100000) __builtin_amdgcn_s_sleep(1);
#endif
}
extern "C" void kernel_launch(void* const* d_in, const int* in_sizes, int n_in, void* d_out, int out_size,
                              void* d_ws, size_t ws_size, hipStream_t stream) {
    (void)in_sizes; (void)n_in; (void)out_size; (void)ws_size;
    if (USEK0) fnet_k0<<<1, 64, 0, stream>>>((char*)d_ws);
    fnet_k1<<<256, 384, 0, stream>>>(d_in[0], d_in[1], d_in[2], (char*)d_ws);
    fnet_k2<<<256, 384, 0, stream>>>((char*)d_ws, d_in[3], d_in[4], (char*)d_out);
    fnet_k3<<<1, 64, 0, stream>>>((char*)d_ws);
}
```

```cpp
#define CH 4
#define PFCODE 104
#include <hip/hip_runtime.h>
#include <hip/hip_bf16.h>

typedef __attribute__((ext_vector_type(2))) __bf16 bf16x2;
typedef __attribute__((ext_vector_type(2))) float f32x2;
typedef __attribute__((ext_vector_type(4))) float f32x4;
typedef __attribute__((ext_vector_type(4))) unsigned u32x4;
typedef __attribute__((ext_vector_type(2))) unsigned u32x2;
typedef unsigned long long u64;
#define LAS __attribute__((address_space(3)))
#define GAS __attribute__((address_space(1)))

constexpr int S_ = 2048, D_ = 640;
constexpr int PQ_BYTES = 128 * 80 * 1024;
constexpr int FLAG_OFF = PQ_BYTES;
constexpr int EPOCH_OFF = FLAG_OFF + 256 * 128;
#define BAL 1
#define YAUX 0
#define STAMP(i)

#if defined(__HIP_DEVICE_COMPILE__)
typedef __amdgpu_buffer_rsrc_t rsrc_t;
__device__ __forceinline__ u64 rt() { u64 t; asm volatile("s_memrealtime %0\n\ts_waitcnt lgkmcnt(0)" : "=s"(t) :: "memory"); return t; }
template <int N> __device__ __forceinline__ void wait_vm() { asm volatile("s_waitcnt vmcnt(%0)" ::"i"(N) : "memory"); }
__device__ __forceinline__ void wait_lds() { asm volatile("s_waitcnt lgkmcnt(0)" ::: "memory"); }
__device__ __forceinline__ void wg_barrier() { asm volatile("s_waitcnt lgkmcnt(0)\n\ts_barrier" ::: "memory"); }
__device__ __forceinline__ rsrc_t mk_rsrc(const void* p, unsigned bytes) {
    return __builtin_amdgcn_make_buffer_rsrc((void*)p, 0, bytes, 0x00020000);
}
__device__ __forceinline__ void dma16(rsrc_t r, LAS char* dst, unsigned voff, unsigned soff) {
    __builtin_amdgcn_raw_ptr_buffer_load_lds(r, (LAS void*)dst, 16, voff, soff, 0, 0);
}
__device__ __forceinline__ unsigned pack2(float a, float b) {
    f32x2 v = {a, b};
    return __builtin_bit_cast(unsigned, __builtin_convertvector(v, bf16x2));
}
__device__ __forceinline__ u32x4 pack_pair(f32x4 a, f32x4 b) {
    unsigned a0 = pack2(a[0], a[1]), a1 = pack2(a[2], a[3]);
    unsigned b0 = pack2(b[0], b[1]), b1 = pack2(b[2], b[3]);
    auto s0 = __builtin_amdgcn_permlane32_swap(a0, b0, false, false);
    auto s1 = __builtin_amdgcn_permlane32_swap(a1, b1, false, false);
    auto t0 = __builtin_amdgcn_permlane16_swap(s0[0], s0[1], false, false);
    auto t1 = __builtin_amdgcn_permlane16_swap(s1[0], s1[1], false, false);
    u32x4 r = {t0[0], t1[0], t0[1], t1[1]};
    return r;
}
#define PIN5(a) asm volatile("" : "+v"(a[0]), "+v"(a[1]), "+v"(a[2]), "+v"(a[3]), "+v"(a[4]))
#define MFMA(acc, a, b) asm volatile("v_mfma_f32_16x16x32_bf16 %0, %1, %2, %0" : "+v"(acc) : "v"(a), "v"(b))
#define RD128(d, ad, off) asm volatile("ds_read_b128 %0, %1 offset:%2" : "=v"(d) : "v"(ad), "i"(off))
#define RDTR(d, ad, off) asm volatile("ds_read_b64_tr_b16 %0, %1 offset:%2" : "=v"(d) : "v"(ad), "i"(off))
__device__ __forceinline__ u64 ld_agent(const char* p) {
    return __hip_atomic_load((GAS u64*)p, __ATOMIC_RELAXED, __HIP_MEMORY_SCOPE_AGENT);
}
__device__ __forceinline__ void st_agent(char* p, u64 v) {
    __hip_atomic_store((GAS u64*)p, v, __ATOMIC_RELAXED, __HIP_MEMORY_SCOPE_AGENT);
}

constexpr int P1_AB = 16384, P1_SLOT = 36864, P1_STEPS = 16;
constexpr int W_SLOT = 10240;
constexpr int SPIN_CAP = 1 << 20;

__device__ __forceinline__ void loader(LAS char* smem, const void* x, const void* cr, const void* ci, const void* wre,
                                       const void* wim, char* ws, int tb, int nj, int half, int off, int et, int wrot,
                                       int lw, int lane) {
    rsrc_t rr = mk_rsrc((const char*)cr + (size_t)tb * (64 * S_ * 2) + half * 2048, 64 * S_ * 2 - half * 2048);
    rsrc_t ri = mk_rsrc((const char*)ci + (size_t)tb * (64 * S_ * 2) + half * 2048, 64 * S_ * 2 - half * 2048);
    rsrc_t rx = mk_rsrc((const char*)x + half * (1024 * D_ * 2) + nj * 320, 1024 * D_ * 2 - nj * 320);
    const int row = 8 * lw + (lane >> 3);
    const unsigned swz = ((lane & 7) ^ ((row >> 1) & 7)) << 4;
    const unsigned voA = row * (S_ * 2) + swz;
    auto kappa = [&](int s) { return (off + (16 / CH) * (s % CH) + s / CH) & 15; };
    auto issue_c = [&](int s, int slot) {
        LAS char* sb = smem + slot * P1_SLOT + lw * 1024;
        const int kap = kappa(s);
#pragma unroll
        for (int g = 0; g < 8; ++g) dma16((g & 1) ? ri : rr, sb + g * 2048, voA, kap * 128 + (g >> 1) * (16 * S_ * 2));
    };
    issue_c(0, 2);
    __builtin_amdgcn_sched_barrier(0);
    unsigned voB[5];
#pragma unroll
    for (int b = 0; b < 5; ++b) {
        int c = b * 64 + lane, kr = c / 20, pos = c - kr * 20;
        voB[b] = (kr + 32 * lw) * (D_ * 2) + ((pos ^ (((kr >> 3) & 1) << 1)) << 4);
    }
    auto issue_x = [&](int s, int slot) {
        LAS char* sb = smem + slot * P1_SLOT + P1_AB + lw * 10240;
        const int kap = kappa(s);
#pragma unroll
        for (int b = 0; b < 10; ++b)
            dma16(rx, sb + b * 1024, voB[b % 5], kap * (64 * D_ * 2) + (b / 5) * (16 * D_ * 2));
    };
    issue_x(0, 2);
    issue_c(1, 0); issue_x(1, 0);
    const char* pre = (const char*)wre + et * 80 * (D_ * 2);
    const char* pim = (const char*)wim + et * 80 * (D_ * 2);
    const unsigned voW = row * (D_ * 2) + swz;
    auto issue_w = [&](int u) {
        int sg = u + wrot;
        sg = sg >= 20 ? sg - 20 : sg;
        rsrc_t r = mk_rsrc(sg < 10 ? pre : pim, 80 * D_ * 2);
        const int ks = sg < 10 ? sg : sg - 10;
        LAS char* sb = smem + ((u + 11) & 15) * W_SLOT + lw * 1024;
#pragma unroll
        for (int j = 0; j < 5; ++j) dma16(r, sb + j * 2048, voW, j * 16 * (D_ * 2) + ks * 128);
    };
    int slot = 1;
    auto adv = [&]() { int r = slot; slot = (slot == 2) ? 0 : slot + 1; return r; };
#pragma unroll 1
    for (int s = 0; s < 10; s += 2) {
        int r;
        wait_vm<18>(); wg_barrier();
        r = adv(); issue_c(s + 2, r); issue_x(s + 2, r); issue_w(s >> 1);
        wait_vm<23>(); wg_barrier();
        r = adv(); issue_c(s + 3, r); issue_x(s + 3, r);
    }
#pragma unroll 1
    for (int s = 10; s < 14; ++s) {
        wait_vm<18>(); wg_barrier();
        int r = adv(); issue_c(s + 2, r); issue_x(s + 2, r);
    }
    wait_vm<18>(); wg_barrier();
#pragma unroll 1
    for (int u = 5; u < 8; ++u) issue_w(u);
    wait_vm<15>(); wg_barrier();
#pragma unroll 1
    for (int u = 8; u < 12; ++u) issue_w(u);
    wg_barrier();
#pragma unroll 1
    for (int u = 12; u < 18; ++u) issue_w(u);
    wg_barrier();
    wait_vm<0>(); wg_barrier();
    wg_barrier();
    issue_w(18); issue_w(19);
    wait_vm<0>(); wg_barrier();
    if (blockIdx.x == 0 && lw == 0) {
        const u64 tag = ld_agent(ws + EPOCH_OFF) + 1;
#pragma unroll 1
        for (int spin = 0; spin < SPIN_CAP; ++spin) {
            bool ok = true;
#pragma unroll
            for (int i = 0; i < 4; ++i) ok &= ld_agent(ws + FLAG_OFF + (i * 64 + lane) * 128) == tag;
            if (__all(ok)) break;
            __builtin_amdgcn_s_sleep(4);
        }
        if (lane == 0) st_agent(ws + EPOCH_OFF, tag);
    }
}

struct XF { u32x2 lo, hi; };
struct P1Frags { u32x4 c[4]; XF x[5]; };
__device__ __forceinline__ u32x4 cat(const XF& x) { return u32x4{x.lo[0], x.lo[1], x.hi[0], x.hi[1]}; }

template <int KS> __device__ __forceinline__ void p1_rd(int j, P1Frags& n, unsigned a, unsigned b0, unsigned b1, unsigned b4) {
    constexpr int KO = KS * 10240;
    if (j == 0) RDTR(n.x[0].lo, b0, KO);
    if (j == 1) RDTR(n.x[0].hi, b0, KO + 1280);
    if (j == 2) RD128(n.c[0], a, 0);
    if (j == 3) RD128(n.c[1], a, 2048);
    if (j == 4) RD128(n.c[2], a, 4096);
    if (j == 5) RD128(n.c[3], a, 6144);
    if (j == 6) RDTR(n.x[1].lo, b1, KO + 32);
    if (j == 7) RDTR(n.x[1].hi, b1, KO + 32 + 1280);
    if (j == 8) RDTR(n.x[2].lo, b0, KO + 64);
    if (j == 9) RDTR(n.x[2].hi, b0, KO + 64 + 1280);
    if (j == 10) RDTR(n.x[3].lo, b1, KO + 96);
    if (j == 11) RDTR(n.x[3].hi, b1, KO + 96 + 1280);
    if (j == 12) RDTR(n.x[4].lo, b4, KO);
    if (j == 13) RDTR(n.x[4].hi, b4, KO + 1280);
}
template <int KS, bool RD>
__device__ __forceinline__ void p1_half(f32x4 (&acc)[4][5], const P1Frags& f, P1Frags& n, unsigned a, unsigned b0, unsigned b1, unsigned b4) {
#pragma unroll
    for (int i = 0; i < 5; ++i)
#pragma unroll
        for (int g = 0; g < 4; ++g) {
            u32x4 xx = cat(f.x[i]);
            MFMA(acc[g][i], xx, f.c[g]);
            if (RD) p1_rd<KS>(i * 4 + g, n, a, b0, b1, b4);
        }
}
struct WF { u32x4 w[2][5]; };
template <bool RD, bool LD>
__device__ __forceinline__ void p2_stage(f32x4 (&acc)[5], WF& f, u32x4 (&a)[4], unsigned o0, unsigned o1,
                                         rsrc_t ra, unsigned va, unsigned sa) {
#pragma unroll
    for (int i = 0; i < 4; ++i) {
#pragma unroll
        for (int j = 0; j < 5; ++j) {
            MFMA(acc[j], f.w[i >> 1][j], a[i]);
            if (RD && i == 1) RD128(f.w[0][j], o0, j * 2048);
            if (RD && i == 3) RD128(f.w[1][j], o1, j * 2048);
        }
        if (LD) a[i] = __builtin_amdgcn_raw_buffer_load_b128(ra, va + i * 1024, sa, 16);
    }
}
template <int N> __device__ __forceinline__ void wait_ldsn() { asm volatile("s_waitcnt lgkmcnt(%0)" ::"i"(N) : "memory"); }

__device__ __forceinline__ void compute(LAS char* smem, char* ws, char* y, int tb, int nj, int half, int tb2, int et,
                                        int wrot, int w, int lane
) {
    const u64 epoch = ld_agent(ws + EPOCH_OFF);
    const unsigned base = (unsigned)(size_t)smem;
    const int wm = w >> 1, wn = w & 1;
    const int m = lane & 15, g4 = lane >> 4, q = (lane & 15) >> 2, p = lane & 3;
    const unsigned o0 = base + m * 128 + ((g4 ^ ((m >> 1) & 7)) << 4), o1 = o0 ^ 64;
    rsrc_t rpq = mk_rsrc(ws, PQ_BYTES);
    WF w0, w1;
    {
        const unsigned oa0 = o0 + wm * 8192, oa1 = o1 + wm * 8192;
        const unsigned rowb = base + P1_AB + (8 * g4 + q) * 320 + (p >> 1) * 16 + (p & 1) * 8;
        const int sw = (g4 & 1) * 32;
        const unsigned ob0 = rowb + wn * 192 + sw, ob1 = rowb + wn * 192 - sw, ob4 = rowb + (((4 + wn) * 32) ^ sw);
        f32x4 acc[4][5];
#pragma unroll
        for (int g = 0; g < 4; ++g)
#pragma unroll
            for (int i = 0; i < 5; ++i) acc[g][i] = f32x4{0, 0, 0, 0};
        PIN5(acc[0]); PIN5(acc[1]); PIN5(acc[2]); PIN5(acc[3]);
        P1Frags f0, f1;
        unsigned so = 2 * P1_SLOT;
        if (w == 0) STAMP(1);
        wg_barrier();
        if (w == 0) STAMP(2);
#pragma unroll
        for (int j = 0; j < 14; ++j) p1_rd<0>(j, f0, oa0 + so, ob0 + so, ob1 + so, ob4 + so);
#pragma unroll 1
        for (int s = 0; s < P1_STEPS - 1; ++s) {
            wait_lds();
            p1_half<1, true>(acc, f0, f1, oa1 + so, ob0 + so, ob1 + so, ob4 + so);
            so = (so == 2 * P1_SLOT) ? 0 : so + P1_SLOT;
            wg_barrier();
            p1_half<0, true>(acc, f1, f0, oa0 + so, ob0 + so, ob1 + so, ob4 + so);
        }
        wait_lds();
        p1_half<1, true>(acc, f0, f1, oa1 + so, ob0 + so, ob1 + so, ob4 + so);
        wait_lds();
        {
            const unsigned u0 = o0 + 11 * W_SLOT, u1 = o1 + 11 * W_SLOT;
#pragma unroll
            for (int i = 0; i < 5; ++i)
#pragma unroll
                for (int g = 0; g < 4; ++g) {
                    u32x4 xx = cat(f1.x[i]);
                    MFMA(acc[g][i], xx, f1.c[g]);
                    if (g == 0) RD128(w0.w[0][i], u0, i * 2048);
                    if (g == 1) RD128(w0.w[1][i], u1, i * 2048);
                    if (g == 2) RD128(w1.w[0][i], u0, W_SLOT + i * 2048);
                    if (g == 3) RD128(w1.w[1][i], u1, W_SLOT + i * 2048);
                }
        }
        asm volatile("s_nop 7\n\ts_nop 7" : "+v"(acc[0][0]), "+v"(acc[0][1]), "+v"(acc[0][2]), "+v"(acc[0][3]), "+v"(acc[0][4]),
                     "+v"(acc[1][0]), "+v"(acc[1][1]), "+v"(acc[1][2]), "+v"(acc[1][3]), "+v"(acc[1][4]),
                     "+v"(acc[2][0]), "+v"(acc[2][1]), "+v"(acc[2][2]), "+v"(acc[2][3]), "+v"(acc[2][4]),
                     "+v"(acc[3][0]), "+v"(acc[3][1]), "+v"(acc[3][2]), "+v"(acc[3][3]), "+v"(acc[3][4]));
        wait_lds();
        if (w == 0) STAMP(18);
        const unsigned sb = (((4 * tb + 2 * wm) * 40 + nj * 5) * 2 + half) * 1024;
        const unsigned v16 = wn * 6144 + lane * 16, v8 = 4096 + wn * 512 + (g4 >> 1) * 256 + m * 16 + (g4 & 1) * 8;
#pragma unroll
        for (int g = 0; g < 4; ++g) {
            const unsigned sg = (g & 1) ? 0x80008000u : 0u;
            const unsigned so2 = sb + ((g >> 1) * 40 + (g & 1) * 20) * 2048;
            u32x4 v0 = pack_pair(acc[g][0], acc[g][1]) ^ u32x4{sg, sg, sg, sg};
            u32x4 v1 = pack_pair(acc[g][2], acc[g][3]) ^ u32x4{sg, sg, sg, sg};
            u32x2 v2 = {pack2(acc[g][4][0], acc[g][4][1]) ^ sg, pack2(acc[g][4][2], acc[g][4][3]) ^ sg};
            __builtin_amdgcn_raw_buffer_store_b128(v0, rpq, v16, so2, 16);
            __builtin_amdgcn_raw_buffer_store_b128(v1, rpq, v16 + 2048, so2, 16);
            __builtin_amdgcn_raw_buffer_store_b64(v2, rpq, v8, so2, 16);
        }
        wg_barrier();
        wait_vm<0>();
        wg_barrier();
        if (w == 0) STAMP(19);
    }
    if (w == 0) {
        const u64 tag = epoch + 1;
        if (lane == 0) st_agent(ws + FLAG_OFF + (tb * 8 + half * 4 + nj) * 128, tag);
        const char* fp = ws + FLAG_OFF + (tb2 * 8 + (lane & 7)) * 128;
#pragma unroll 1
        for (int spin = 0; spin < SPIN_CAP; ++spin) {
            if (__all(ld_agent(fp) == tag)) break;
            __builtin_amdgcn_s_sleep(1);
        }
        STAMP(20);
    }
    f32x4 acc[5];
#pragma unroll
    for (int j = 0; j < 5; ++j) acc[j] = f32x4{0, 0, 0, 0};
    PIN5(acc);
    wg_barrier();
    if (w == 0) STAMP(21);
    const int tg = tb2 * 4 + w;
    const unsigned va = lane * 16, sa = tg * 81920;
    unsigned ao = wrot * 4096;
    auto anext = [&]() { unsigned r = sa + ao; ao = (ao == 19 * 4096) ? 0 : ao + 4096; return r; };
    u32x4 a0[4], a1[4], a2[4], a3[4];
    { unsigned r = anext();
#pragma unroll
      for (int i = 0; i < 4; ++i) a0[i] = __builtin_amdgcn_raw_buffer_load_b128(rpq, va + i * 1024, r, 16); }
    { unsigned r = anext();
#pragma unroll
      for (int i = 0; i < 4; ++i) a1[i] = __builtin_amdgcn_raw_buffer_load_b128(rpq, va + i * 1024, r, 16); }
    { unsigned r = anext();
#pragma unroll
      for (int i = 0; i < 4; ++i) a2[i] = __builtin_amdgcn_raw_buffer_load_b128(rpq, va + i * 1024, r, 16); }
    { unsigned r = anext();
#pragma unroll
      for (int i = 0; i < 4; ++i) a3[i] = __builtin_amdgcn_raw_buffer_load_b128(rpq, va + i * 1024, r, 16); }
    unsigned wo = 13 * W_SLOT;
    auto nxt = [&]() { unsigned r = wo; wo = (wo == 15 * W_SLOT) ? 0 : wo + W_SLOT; return r; };
#pragma unroll 1
    for (int it = 0; it < 4; ++it) {
        unsigned r;
        if (it == 1) wg_barrier();
        wait_ldsn<10>(); r = nxt(); p2_stage<true, true>(acc, w0, a0, o0 + r, o1 + r, rpq, va, anext());
        wait_ldsn<10>(); r = nxt(); p2_stage<true, true>(acc, w1, a1, o0 + r, o1 + r, rpq, va, anext());
        wait_ldsn<10>(); r = nxt(); p2_stage<true, true>(acc, w0, a2, o0 + r, o1 + r, rpq, va, anext());
        wait_ldsn<10>(); r = nxt(); p2_stage<true, true>(acc, w1, a3, o0 + r, o1 + r, rpq, va, anext());
    }
    wg_barrier();
    p2_stage<true, false>(acc, w0, a0, o0 + 13 * W_SLOT, o1 + 13 * W_SLOT, rpq, va, 0);
    p2_stage<true, false>(acc, w1, a1, o0 + 14 * W_SLOT, o1 + 14 * W_SLOT, rpq, va, 0);
    wait_ldsn<10>(); p2_stage<false, false>(acc, w0, a2, 0, 0, rpq, va, 0);
    wait_ldsn<0>(); p2_stage<false, false>(acc, w1, a3, 0, 0, rpq, va, 0);
    asm volatile("s_nop 7\n\ts_nop 7" : "+v"(acc[0]), "+v"(acc[1]), "+v"(acc[2]), "+v"(acc[3]), "+v"(acc[4]));
    if (w == 0) STAMP(26);
    rsrc_t ry = mk_rsrc(y, S_ * D_ * 2);
    const unsigned vy = m * (D_ * 2) + g4 * 16, sy = tg * (16 * D_ * 2) + et * 160;
    __builtin_amdgcn_raw_buffer_store_b128(pack_pair(acc[0], acc[1]), ry, vy, sy, YAUX);
    __builtin_amdgcn_raw_buffer_store_b128(pack_pair(acc[2], acc[3]), ry, vy + 64, sy, YAUX);
    __builtin_amdgcn_raw_buffer_store_b64(u32x2{pack2(acc[4][0], acc[4][1]), pack2(acc[4][2], acc[4][3])}, ry,
                                          m * (D_ * 2) + 128 + g4 * 8, sy, YAUX);
}
#endif

__global__ __launch_bounds__(384, 2) void fnet_fused(const void* __restrict__ x, const void* __restrict__ cr,
                                                     const void* __restrict__ ci, const void* __restrict__ wre,
                                                     const void* __restrict__ wim, char* ws, char* __restrict__ y) {
#if defined(__HIP_DEVICE_COMPILE__)
    __shared__ __attribute__((aligned(1024))) char smem_[163840];
    LAS char* smem = (LAS char*)smem_;
    const int bid = blockIdx.x, xcd = bid & 7, idx = bid >> 3;
    const int half = xcd & 1, tbl = idx >> 2, nj = idx & 3, tb = 8 * (xcd >> 1) + tbl;
    const int tb2 = 4 * xcd + (idx >> 3), et = idx & 7;
    const int lane = threadIdx.x & 63;
    const int wid = __builtin_amdgcn_readfirstlane(threadIdx.x >> 6);
    const int off = CH == 1 ? 0 : BAL ? (16 / CH) * ((nj + tbl) % CH) : (4 * nj + 2 * tbl) & 15;
    const int wrot = 5 * (idx >> 3);
    if (wid >= 4) {
        loader(smem, x, cr, ci, wre, wim, ws, tb, nj, half, off, et, wrot, wid - 4, lane);
    } else {
        if (wid == 3) {
            u64 pc;
            asm volatile("s_getpc_b64 %0" : "=s"(pc));
            pc &= ~255ull;
            unsigned v0, v1, o0 = lane * 128, o1 = (lane < PFCODE - 64 ? lane + 64 : lane) * 128;
            asm volatile("global_load_dword %0, %2, %4\n\tglobal_load_dword %1, %3, %4\n\ts_waitcnt vmcnt(0)"
                         : "=&v"(v0), "=&v"(v1) : "v"(o0), "v"(o1), "s"(pc) : "memory");
        }
        compute(smem, ws, y, tb, nj, half, tb2, et, wrot, wid, lane);
    }
#endif
}

extern "C" void kernel_launch(void* const* d_in, const int* in_sizes, int n_in, void* d_out, int out_size,
                              void* d_ws, size_t ws_size, hipStream_t stream) {
    (void)in_sizes; (void)n_in; (void)out_size; (void)ws_size;
    fnet_fused<<<256, 384, 0, stream>>>(d_in[0], d_in[1], d_in[2], d_in[3], d_in[4], (char*)d_ws, (char*)d_out);
}
```

```cpp
#define AMPT 0
#include <hip/hip_runtime.h>
#include <hip/hip_bf16.h>

typedef __attribute__((ext_vector_type(2))) __bf16 bf16x2;
typedef __attribute__((ext_vector_type(2))) float f32x2;
typedef __attribute__((ext_vector_type(4))) float f32x4;
typedef __attribute__((ext_vector_type(4))) unsigned u32x4;
typedef __attribute__((ext_vector_type(2))) unsigned u32x2;
typedef unsigned long long u64;
#define LAS __attribute__((address_space(3)))
#define GAS __attribute__((address_space(1)))

constexpr int S_ = 2048, D_ = 640;
constexpr int PQ_BYTES = 128 * 80 * 1024;
constexpr int FLAG_OFF = PQ_BYTES;
constexpr int EPOCH_OFF = FLAG_OFF + 256 * 128;
#define CH 4
#define BAL 1
#define PFCODE 104
#define YAUX 0
#define STAMP(i) do { if ((i) == AMPT) tamp = rt(); } while (0)

#if defined(__HIP_DEVICE_COMPILE__)
typedef __amdgpu_buffer_rsrc_t rsrc_t;
__device__ __forceinline__ u64 rt() { u64 t; asm volatile("s_memrealtime %0\n\ts_waitcnt lgkmcnt(0)" : "=s"(t) :: "memory"); return t; }
template <int N> __device__ __forceinline__ void wait_vm() { asm volatile("s_waitcnt vmcnt(%0)" ::"i"(N) : "memory"); }
__device__ __forceinline__ void wait_lds() { asm volatile("s_waitcnt lgkmcnt(0)" ::: "memory"); }
__device__ __forceinline__ void wg_barrier() { asm volatile("s_waitcnt lgkmcnt(0)\n\ts_barrier" ::: "memory"); }
__device__ __forceinline__ rsrc_t mk_rsrc(const void* p, unsigned bytes) {
    return __builtin_amdgcn_make_buffer_rsrc((void*)p, 0, bytes, 0x00020000);
}
__device__ __forceinline__ void dma16(rsrc_t r, LAS char* dst, unsigned voff, unsigned soff) {
    __builtin_amdgcn_raw_ptr_buffer_load_lds(r, (LAS void*)dst, 16, voff, soff, 0, 0);
}
__device__ __forceinline__ unsigned pack2(float a, float b) {
    f32x2 v = {a, b};
    return __builtin_bit_cast(unsigned, __builtin_convertvector(v, bf16x2));
}
__device__ __forceinline__ u32x4 pack_pair(f32x4 a, f32x4 b) {
    unsigned a0 = pack2(a[0], a[1]), a1 = pack2(a[2], a[3]);
    unsigned b0 = pack2(b[0], b[1]), b1 = pack2(b[2], b[3]);
    auto s0 = __builtin_amdgcn_permlane32_swap(a0, b0, false, false);
    auto s1 = __builtin_amdgcn_permlane32_swap(a1, b1, false, false);
    auto t0 = __builtin_amdgcn_permlane16_swap(s0[0], s0[1], false, false);
    auto t1 = __builtin_amdgcn_permlane16_swap(s1[0], s1[1], false, false);
    u32x4 r = {t0[0], t1[0], t0[1], t1[1]};
    return r;
}
#define PIN5(a) asm volatile("" : "+v"(a[0]), "+v"(a[1]), "+v"(a[2]), "+v"(a[3]), "+v"(a[4]))
#define MFMA(acc, a, b) asm volatile("v_mfma_f32_16x16x32_bf16 %0, %1, %2, %0" : "+v"(acc) : "v"(a), "v"(b))
#define RD128(d, ad, off) asm volatile("ds_read_b128 %0, %1 offset:%2" : "=v"(d) : "v"(ad), "i"(off))
#define RDTR(d, ad, off) asm volatile("ds_read_b64_tr_b16 %0, %1 offset:%2" : "=v"(d) : "v"(ad), "i"(off))
__device__ __forceinline__ u64 ld_agent(const char* p) {
    return __hip_atomic_load((GAS u64*)p, __ATOMIC_RELAXED, __HIP_MEMORY_SCOPE_AGENT);
}
__device__ __forceinline__ void st_agent(char* p, u64 v) {
    __hip_atomic_store((GAS u64*)p, v, __ATOMIC_RELAXED, __HIP_MEMORY_SCOPE_AGENT);
}

constexpr int P1_AB = 16384, P1_SLOT = 36864, P1_STEPS = 16;
constexpr int W_SLOT = 10240;
constexpr int SPIN_CAP = 1 << 20;

__device__ __forceinline__ void loader(LAS char* smem, const void* x, const void* cr, const void* ci, const void* wre,
                                       const void* wim, char* ws, int tb, int nj, int half, int off, int et, int wrot,
                                       int lw, int lane) {
    rsrc_t rr = mk_rsrc((const char*)cr + (size_t)tb * (64 * S_ * 2) + half * 2048, 64 * S_ * 2 - half * 2048);
    rsrc_t ri = mk_rsrc((const char*)ci + (size_t)tb * (64 * S_ * 2) + half * 2048, 64 * S_ * 2 - half * 2048);
    rsrc_t rx = mk_rsrc((const char*)x + half * (1024 * D_ * 2) + nj * 320, 1024 * D_ * 2 - nj * 320);
    const int row = 8 * lw + (lane >> 3);
    const unsigned swz = ((lane & 7) ^ ((row >> 1) & 7)) << 4;
    const unsigned voA = row * (S_ * 2) + swz;
    auto kappa = [&](int s) { return (off + (16 / CH) * (s % CH) + s / CH) & 15; };
    auto issue_c = [&](int s, int slot) {
        LAS char* sb = smem + slot * P1_SLOT + lw * 1024;
        const int kap = kappa(s);
#pragma unroll
        for (int g = 0; g < 8; ++g) dma16((g & 1) ? ri : rr, sb + g * 2048, voA, kap * 128 + (g >> 1) * (16 * S_ * 2));
    };
    issue_c(0, 2);
    __builtin_amdgcn_sched_barrier(0);
    unsigned voB[5];
#pragma unroll
    for (int b = 0; b < 5; ++b) {
        int c = b * 64 + lane, kr = c / 20, pos = c - kr * 20;
        voB[b] = (kr + 32 * lw) * (D_ * 2) + ((pos ^ (((kr >> 3) & 1) << 1)) << 4);
    }
    auto issue_x = [&](int s, int slot) {
        LAS char* sb = smem + slot * P1_SLOT + P1_AB + lw * 10240;
        const int kap = kappa(s);
#pragma unroll
        for (int b = 0; b < 10; ++b)
            dma16(rx, sb + b * 1024, voB[b % 5], kap * (64 * D_ * 2) + (b / 5) * (16 * D_ * 2));
    };
    issue_x(0, 2);
    issue_c(1, 0); issue_x(1, 0);
    const char* pre = (const char*)wre + et * 80 * (D_ * 2);
    const char* pim = (const char*)wim + et * 80 * (D_ * 2);
    const unsigned voW = row * (D_ * 2) + swz;
    auto issue_w = [&](int u) {
        int sg = u + wrot;
        sg = sg >= 20 ? sg - 20 : sg;
        rsrc_t r = mk_rsrc(sg < 10 ? pre : pim, 80 * D_ * 2);
        const int ks = sg < 10 ? sg : sg - 10;
        LAS char* sb = smem + ((u + 11) & 15) * W_SLOT + lw * 1024;
#pragma unroll
        for (int j = 0; j < 5; ++j) dma16(r, sb + j * 2048, voW, j * 16 * (D_ * 2) + ks * 128);
    };
    int slot = 1;
    auto adv = [&]() { int r = slot; slot = (slot == 2) ? 0 : slot + 1; return r; };
#pragma unroll 1
    for (int s = 0; s < 10; s += 2) {
        int r;
        wait_vm<18>(); wg_barrier();
        r = adv(); issue_c(s + 2, r); issue_x(s + 2, r); issue_w(s >> 1);
        wait_vm<23>(); wg_barrier();
        r = adv(); issue_c(s + 3, r); issue_x(s + 3, r);
    }
#pragma unroll 1
    for (int s = 10; s < 14; ++s) {
        wait_vm<18>(); wg_barrier();
        int r = adv(); issue_c(s + 2, r); issue_x(s + 2, r);
    }
    wait_vm<18>(); wg_barrier();
#pragma unroll 1
    for (int u = 5; u < 8; ++u) issue_w(u);
    wait_vm<15>(); wg_barrier();
#pragma unroll 1
    for (int u = 8; u < 12; ++u) issue_w(u);
    wg_barrier();
#pragma unroll 1
    for (int u = 12; u < 18; ++u) issue_w(u);
    wg_barrier();
    wait_vm<0>(); wg_barrier();
    wg_barrier();
    issue_w(18); issue_w(19);
    wait_vm<0>(); wg_barrier();
    if (blockIdx.x == 0 && lw == 0) {
        const u64 tag = ld_agent(ws + EPOCH_OFF) + 1;
#pragma unroll 1
        for (int spin = 0; spin < SPIN_CAP; ++spin) {
            bool ok = true;
#pragma unroll
            for (int i = 0; i < 4; ++i) ok &= ld_agent(ws + FLAG_OFF + (i * 64 + lane) * 128) == tag;
            if (__all(ok)) break;
            __builtin_amdgcn_s_sleep(4);
        }
        if (lane == 0) st_agent(ws + EPOCH_OFF, tag);
    }
}

struct XF { u32x2 lo, hi; };
struct P1Frags { u32x4 c[4]; XF x[5]; };
__device__ __forceinline__ u32x4 cat(const XF& x) { return u32x4{x.lo[0], x.lo[1], x.hi[0], x.hi[1]}; }

template <int KS> __device__ __forceinline__ void p1_rd(int j, P1Frags& n, unsigned a, unsigned b0, unsigned b1, unsigned b4) {
    constexpr int KO = KS * 10240;
    if (j == 0) RDTR(n.x[0].lo, b0, KO);
    if (j == 1) RDTR(n.x[0].hi, b0, KO + 1280);
    if (j == 2) RD128(n.c[0], a, 0);
    if (j == 3) RD128(n.c[1], a, 2048);
    if (j == 4) RD128(n.c[2], a, 4096);
    if (j == 5) RD128(n.c[3], a, 6144);
    if (j == 6) RDTR(n.x[1].lo, b1, KO + 32);
    if (j == 7) RDTR(n.x[1].hi, b1, KO + 32 + 1280);
    if (j == 8) RDTR(n.x[2].lo, b0, KO + 64);
    if (j == 9) RDTR(n.x[2].hi, b0, KO + 64 + 1280);
    if (j == 10) RDTR(n.x[3].lo, b1, KO + 96);
    if (j == 11) RDTR(n.x[3].hi, b1, KO + 96 + 1280);
    if (j == 12) RDTR(n.x[4].lo, b4, KO);
    if (j == 13) RDTR(n.x[4].hi, b4, KO + 1280);
}
template <int KS, bool RD>
__device__ __forceinline__ void p1_half(f32x4 (&acc)[4][5], const P1Frags& f, P1Frags& n, unsigned a, unsigned b0, unsigned b1, unsigned b4) {
#pragma unroll
    for (int i = 0; i < 5; ++i)
#pragma unroll
        for (int g = 0; g < 4; ++g) {
            u32x4 xx = cat(f.x[i]);
            MFMA(acc[g][i], xx, f.c[g]);
            if (RD) p1_rd<KS>(i * 4 + g, n, a, b0, b1, b4);
        }
}
struct WF { u32x4 w[2][5]; };
template <bool RD, bool LD>
__device__ __forceinline__ void p2_stage(f32x4 (&acc)[5], WF& f, u32x4 (&a)[4], unsigned o0, unsigned o1,
                                         rsrc_t ra, unsigned va, unsigned sa) {
#pragma unroll
    for (int i = 0; i < 4; ++i) {
#pragma unroll
        for (int j = 0; j < 5; ++j) {
            MFMA(acc[j], f.w[i >> 1][j], a[i]);
            if (RD && i == 1) RD128(f.w[0][j], o0, j * 2048);
            if (RD && i == 3) RD128(f.w[1][j], o1, j * 2048);
        }
        if (LD) a[i] = __builtin_amdgcn_raw_buffer_load_b128(ra, va + i * 1024, sa, 16);
    }
}
template <int N> __device__ __forceinline__ void wait_ldsn() { asm volatile("s_waitcnt lgkmcnt(%0)" ::"i"(N) : "memory"); }

__device__ __forceinline__ void compute(LAS char* smem, char* ws, char* y, int tb, int nj, int half, int tb2, int et,
                                        int wrot, int w, int lane
    , u64 t0
) {
    u64 tamp = t0;
    const u64 epoch = ld_agent(ws + EPOCH_OFF);
    const unsigned base = (unsigned)(size_t)smem;
    const int wm = w >> 1, wn = w & 1;
    const int m = lane & 15, g4 = lane >> 4, q = (lane & 15) >> 2, p = lane & 3;
    const unsigned o0 = base + m * 128 + ((g4 ^ ((m >> 1) & 7)) << 4), o1 = o0 ^ 64;
    rsrc_t rpq = mk_rsrc(ws, PQ_BYTES);
    WF w0, w1;
    {
        const unsigned oa0 = o0 + wm * 8192, oa1 = o1 + wm * 8192;
        const unsigned rowb = base + P1_AB + (8 * g4 + q) * 320 + (p >> 1) * 16 + (p & 1) * 8;
        const int sw = (g4 & 1) * 32;
        const unsigned ob0 = rowb + wn * 192 + sw, ob1 = rowb + wn * 192 - sw, ob4 = rowb + (((4 + wn) * 32) ^ sw);
        f32x4 acc[4][5];
#pragma unroll
        for (int g = 0; g < 4; ++g)
#pragma unroll
            for (int i = 0; i < 5; ++i) acc[g][i] = f32x4{0, 0, 0, 0};
        PIN5(acc[0]); PIN5(acc[1]); PIN5(acc[2]); PIN5(acc[3]);
        P1Frags f0, f1;
        unsigned so = 2 * P1_SLOT;
        if (w == 0) STAMP(1);
        wg_barrier();
        if (w == 0) STAMP(2);
#pragma unroll
        for (int j = 0; j < 14; ++j) p1_rd<0>(j, f0, oa0 + so, ob0 + so, ob1 + so, ob4 + so);
#pragma unroll 1
        for (int s = 0; s < P1_STEPS - 1; ++s) {
            wait_lds();
            p1_half<1, true>(acc, f0, f1, oa1 + so, ob0 + so, ob1 + so, ob4 + so);
            so = (so == 2 * P1_SLOT) ? 0 : so + P1_SLOT;
            wg_barrier();
            if (AMPT >= 3 && AMPT < 18 && s == AMPT - 3) tamp = rt();
            p1_half<0, true>(acc, f1, f0, oa0 + so, ob0 + so, ob1 + so, ob4 + so);
        }
        wait_lds();
        p1_half<1, true>(acc, f0, f1, oa1 + so, ob0 + so, ob1 + so, ob4 + so);
        wait_lds();
        {
            const unsigned u0 = o0 + 11 * W_SLOT, u1 = o1 + 11 * W_SLOT;
#pragma unroll
            for (int i = 0; i < 5; ++i)
#pragma unroll
                for (int g = 0; g < 4; ++g) {
                    u32x4 xx = cat(f1.x[i]);
                    MFMA(acc[g][i], xx, f1.c[g]);
                    if (g == 0) RD128(w0.w[0][i], u0, i * 2048);
                    if (g == 1) RD128(w0.w[1][i], u1, i * 2048);
                    if (g == 2) RD128(w1.w[0][i], u0, W_SLOT + i * 2048);
                    if (g == 3) RD128(w1.w[1][i], u1, W_SLOT + i * 2048);
                }
        }
        asm volatile("s_nop 7\n\ts_nop 7" : "+v"(acc[0][0]), "+v"(acc[0][1]), "+v"(acc[0][2]), "+v"(acc[0][3]), "+v"(acc[0][4]),
                     "+v"(acc[1][0]), "+v"(acc[1][1]), "+v"(acc[1][2]), "+v"(acc[1][3]), "+v"(acc[1][4]),
                     "+v"(acc[2][0]), "+v"(acc[2][1]), "+v"(acc[2][2]), "+v"(acc[2][3]), "+v"(acc[2][4]),
                     "+v"(acc[3][0]), "+v"(acc[3][1]), "+v"(acc[3][2]), "+v"(acc[3][3]), "+v"(acc[3][4]));
        wait_lds();
        if (w == 0) STAMP(18);
        const unsigned sb = (((4 * tb + 2 * wm) * 40 + nj * 5) * 2 + half) * 1024;
        const unsigned v16 = wn * 6144 + lane * 16, v8 = 4096 + wn * 512 + (g4 >> 1) * 256 + m * 16 + (g4 & 1) * 8;
#pragma unroll
        for (int g = 0; g < 4; ++g) {
            const unsigned sg = (g & 1) ? 0x80008000u : 0u;
            const unsigned so2 = sb + ((g >> 1) * 40 + (g & 1) * 20) * 2048;
            u32x4 v0 = pack_pair(acc[g][0], acc[g][1]) ^ u32x4{sg, sg, sg, sg};
            u32x4 v1 = pack_pair(acc[g][2], acc[g][3]) ^ u32x4{sg, sg, sg, sg};
            u32x2 v2 = {pack2(acc[g][4][0], acc[g][4][1]) ^ sg, pack2(acc[g][4][2], acc[g][4][3]) ^ sg};
            __builtin_amdgcn_raw_buffer_store_b128(v0, rpq, v16, so2, 16);
            __builtin_amdgcn_raw_buffer_store_b128(v1, rpq, v16 + 2048, so2, 16);
            __builtin_amdgcn_raw_buffer_store_b64(v2, rpq, v8, so2, 16);
        }
        wg_barrier();
        wait_vm<0>();
        wg_barrier();
        if (w == 0) STAMP(19);
    }
    if (w == 0) {
        const u64 tag = epoch + 1;
        if (lane == 0) st_agent(ws + FLAG_OFF + (tb * 8 + half * 4 + nj) * 128, tag);
        const char* fp = ws + FLAG_OFF + (tb2 * 8 + (lane & 7)) * 128;
#pragma unroll 1
        for (int spin = 0; spin < SPIN_CAP; ++spin) {
            if (__all(ld_agent(fp) == tag)) break;
            __builtin_amdgcn_s_sleep(1);
        }
        STAMP(20);
    }
    f32x4 acc[5];
#pragma unroll
    for (int j = 0; j < 5; ++j) acc[j] = f32x4{0, 0, 0, 0};
    PIN5(acc);
    wg_barrier();
    if (w == 0) STAMP(21);
    const int tg = tb2 * 4 + w;
    const unsigned va = lane * 16, sa = tg * 81920;
    unsigned ao = wrot * 4096;
    auto anext = [&]() { unsigned r = sa + ao; ao = (ao == 19 * 4096) ? 0 : ao + 4096; return r; };
    u32x4 a0[4], a1[4], a2[4], a3[4];
    { unsigned r = anext();
#pragma unroll
      for (int i = 0; i < 4; ++i) a0[i] = __builtin_amdgcn_raw_buffer_load_b128(rpq, va + i * 1024, r, 16); }
    { unsigned r = anext();
#pragma unroll
      for (int i = 0; i < 4; ++i) a1[i] = __builtin_amdgcn_raw_buffer_load_b128(rpq, va + i * 1024, r, 16); }
    { unsigned r = anext();
#pragma unroll
      for (int i = 0; i < 4; ++i) a2[i] = __builtin_amdgcn_raw_buffer_load_b128(rpq, va + i * 1024, r, 16); }
    { unsigned r = anext();
#pragma unroll
      for (int i = 0; i < 4; ++i) a3[i] = __builtin_amdgcn_raw_buffer_load_b128(rpq, va + i * 1024, r, 16); }
    unsigned wo = 13 * W_SLOT;
    auto nxt = [&]() { unsigned r = wo; wo = (wo == 15 * W_SLOT) ? 0 : wo + W_SLOT; return r; };
#pragma unroll 1
    for (int it = 0; it < 4; ++it) {
        unsigned r;
        if (it == 1) wg_barrier();
        wait_ldsn<10>(); r = nxt(); p2_stage<true, true>(acc, w0, a0, o0 + r, o1 + r, rpq, va, anext());
        wait_ldsn<10>(); r = nxt(); p2_stage<true, true>(acc, w1, a1, o0 + r, o1 + r, rpq, va, anext());
        wait_ldsn<10>(); r = nxt(); p2_stage<true, true>(acc, w0, a2, o0 + r, o1 + r, rpq, va, anext());
        wait_ldsn<10>(); r = nxt(); p2_stage<true, true>(acc, w1, a3, o0 + r, o1 + r, rpq, va, anext());
    }
    wg_barrier();
    p2_stage<true, false>(acc, w0, a0, o0 + 13 * W_SLOT, o1 + 13 * W_SLOT, rpq, va, 0);
    p2_stage<true, false>(acc, w1, a1, o0 + 14 * W_SLOT, o1 + 14 * W_SLOT, rpq, va, 0);
    wait_ldsn<10>(); p2_stage<false, false>(acc, w0, a2, 0, 0, rpq, va, 0);
    wait_ldsn<0>(); p2_stage<false, false>(acc, w1, a3, 0, 0, rpq, va, 0);
    asm volatile("s_nop 7\n\ts_nop 7" : "+v"(acc[0]), "+v"(acc[1]), "+v"(acc[2]), "+v"(acc[3]), "+v"(acc[4]));
    if (w == 0) STAMP(26);
    rsrc_t ry = mk_rsrc(y, S_ * D_ * 2);
    const unsigned vy = m * (D_ * 2) + g4 * 16, sy = tg * (16 * D_ * 2) + et * 160;
    __builtin_amdgcn_raw_buffer_store_b128(pack_pair(acc[0], acc[1]), ry, vy, sy, YAUX);
    __builtin_amdgcn_raw_buffer_store_b128(pack_pair(acc[2], acc[3]), ry, vy + 64, sy, YAUX);
    __builtin_amdgcn_raw_buffer_store_b64(u32x2{pack2(acc[4][0], acc[4][1]), pack2(acc[4][2], acc[4][3])}, ry,
                                          m * (D_ * 2) + 128 + g4 * 8, sy, YAUX);
    wait_vm<0>();
    if (w == 0) STAMP(27);
    if (blockIdx.x == 100 && w == 0) { u64 d = (tamp - t0) * 10, t = rt(); while (rt() - t < d && d < 100000) __builtin_amdgcn_s_sleep(1); }
}
#endif

__global__ __launch_bounds__(384, 2) void fnet_fused(const void* __restrict__ x, const void* __restrict__ cr,
                                                     const void* __restrict__ ci, const void* __restrict__ wre,
                                                     const void* __restrict__ wim, char* ws, char* __restrict__ y) {
#if defined(__HIP_DEVICE_COMPILE__)
    __shared__ __attribute__((aligned(1024))) char smem_[163840];
    LAS char* smem = (LAS char*)smem_;
    u64 t0 = rt();
    const int bid = blockIdx.x, xcd = bid & 7, idx = bid >> 3;
    const int half = xcd & 1, tbl = idx >> 2, nj = idx & 3, tb = 8 * (xcd >> 1) + tbl;
    const int tb2 = 4 * xcd + (idx >> 3), et = idx & 7;
    const int lane = threadIdx.x & 63;
    const int wid = __builtin_amdgcn_readfirstlane(threadIdx.x >> 6);
    const int off = CH == 1 ? 0 : BAL ? (16 / CH) * ((nj + tbl) % CH) : (4 * nj + 2 * tbl) & 15;
    const int wrot = 5 * (idx >> 3);
    if (wid >= 4) {
        loader(smem, x, cr, ci, wre, wim, ws, tb, nj, half, off, et, wrot, wid - 4, lane);
    } else {
        if (wid == 3) {
            u64 pc;
            asm volatile("s_getpc_b64 %0" : "=s"(pc));
            pc &= ~255ull;
            unsigned v0, v1, o0 = lane * 128, o1 = (lane < PFCODE - 64 ? lane + 64 : lane) * 128;
            asm volatile("global_load_dword %0, %2, %4\n\tglobal_load_dword %1, %3, %4\n\ts_waitcnt vmcnt(0)"
                         : "=&v"(v0), "=&v"(v1) : "v"(o0), "v"(o1), "s"(pc) : "memory");
        }
        compute(smem, ws, y, tb, nj, half, tb2, et, wrot, wid, lane, t0);
    }
#endif
}

extern "C" void kernel_launch(void* const* d_in, const int* in_sizes, int n_in, void* d_out, int out_size,
                              void* d_ws, size_t ws_size, hipStream_t stream) {
    (void)in_sizes; (void)n_in; (void)out_size; (void)ws_size;
    fnet_fused<<<256, 384, 0, stream>>>(d_in[0], d_in[1], d_in[2], d_in[3], d_in[4], (char*)d_ws, (char*)d_out);
}
```

```cpp
#include <hip/hip_runtime.h>
#include <hip/hip_bf16.h>

typedef __attribute__((ext_vector_type(2))) __bf16 bf16x2;
typedef __attribute__((ext_vector_type(2))) float f32x2;
typedef __attribute__((ext_vector_type(4))) float f32x4;
typedef __attribute__((ext_vector_type(4))) unsigned u32x4;
typedef __attribute__((ext_vector_type(2))) unsigned u32x2;
typedef unsigned long long u64;
#define LAS __attribute__((address_space(3)))
#define GAS __attribute__((address_space(1)))

constexpr int S_ = 2048, D_ = 640;
constexpr int PQ_BYTES = 128 * 80 * 1024;
constexpr int FLAG_OFF = PQ_BYTES;
constexpr int EPOCH_OFF = FLAG_OFF + 256 * 128;
#define STAG 1
#define PFCODE 100
#define YAUX 0
#define STAMP(i)

#if defined(__HIP_DEVICE_COMPILE__)
typedef __amdgpu_buffer_rsrc_t rsrc_t;
__device__ __forceinline__ u64 rt() { u64 t; asm volatile("s_memrealtime %0\n\ts_waitcnt lgkmcnt(0)" : "=s"(t) :: "memory"); return t; }
template <int N> __device__ __forceinline__ void wait_vm() { asm volatile("s_waitcnt vmcnt(%0)" ::"i"(N) : "memory"); }
__device__ __forceinline__ void wait_lds() { asm volatile("s_waitcnt lgkmcnt(0)" ::: "memory"); }
__device__ __forceinline__ void wg_barrier() { asm volatile("s_waitcnt lgkmcnt(0)\n\ts_barrier" ::: "memory"); }
__device__ __forceinline__ rsrc_t mk_rsrc(const void* p, unsigned bytes) {
    return __builtin_amdgcn_make_buffer_rsrc((void*)p, 0, bytes, 0x00020000);
}
__device__ __forceinline__ void dma16(rsrc_t r, LAS char* dst, unsigned voff, unsigned soff) {
    __builtin_amdgcn_raw_ptr_buffer_load_lds(r, (LAS void*)dst, 16, voff, soff, 0, 0);
}
__device__ __forceinline__ unsigned pack2(float a, float b) {
    f32x2 v = {a, b};
    return __builtin_bit_cast(unsigned, __builtin_convertvector(v, bf16x2));
}
__device__ __forceinline__ u32x4 pack_pair(f32x4 a, f32x4 b) {
    unsigned a0 = pack2(a[0], a[1]), a1 = pack2(a[2], a[3]);
    unsigned b0 = pack2(b[0], b[1]), b1 = pack2(b[2], b[3]);
    auto s0 = __builtin_amdgcn_permlane32_swap(a0, b0, false, false);
    auto s1 = __builtin_amdgcn_permlane32_swap(a1, b1, false, false);
    auto t0 = __builtin_amdgcn_permlane16_swap(s0[0], s0[1], false, false);
    auto t1 = __builtin_amdgcn_permlane16_swap(s1[0], s1[1], false, false);
    u32x4 r = {t0[0], t1[0], t0[1], t1[1]};
    return r;
}
#define PIN5(a) asm volatile("" : "+v"(a[0]), "+v"(a[1]), "+v"(a[2]), "+v"(a[3]), "+v"(a[4]))
#define MFMA(acc, a, b) asm volatile("v_mfma_f32_16x16x32_bf16 %0, %1, %2, %0" : "+v"(acc) : "v"(a), "v"(b))
#define RD128(d, ad, off) asm volatile("ds_read_b128 %0, %1 offset:%2" : "=v"(d) : "v"(ad), "i"(off))
#define RDTR(d, ad, off) asm volatile("ds_read_b64_tr_b16 %0, %1 offset:%2" : "=v"(d) : "v"(ad), "i"(off))
__device__ __forceinline__ u64 ld_agent(const char* p) {
    return __hip_atomic_load((GAS u64*)p, __ATOMIC_RELAXED, __HIP_MEMORY_SCOPE_AGENT);
}
__device__ __forceinline__ void st_agent(char* p, u64 v) {
    __hip_atomic_store((GAS u64*)p, v, __ATOMIC_RELAXED, __HIP_MEMORY_SCOPE_AGENT);
}

constexpr int P1_AB = 16384, P1_SLOT = 36864, P1_STEPS = 16;
constexpr int W_SLOT = 10240;
constexpr int SPIN_CAP = 1 << 20;

__device__ __forceinline__ void loader(LAS char* smem, const void* x, const void* cr, const void* ci, const void* wre,
                                       const void* wim, char* ws, int tb, int nj, int half, int sig, int gj, int et, int wrot,
                                       int lw, int lane) {
    rsrc_t rr = mk_rsrc((const char*)cr + (size_t)tb * (64 * S_ * 2) + half * 2048, 64 * S_ * 2 - half * 2048);
    rsrc_t ri = mk_rsrc((const char*)ci + (size_t)tb * (64 * S_ * 2) + half * 2048, 64 * S_ * 2 - half * 2048);
    rsrc_t rx = mk_rsrc((const char*)x + half * (1024 * D_ * 2) + nj * 320, 1024 * D_ * 2 - nj * 320);
    const int row = 8 * lw + (lane >> 3);
    const unsigned swz = ((lane & 7) ^ ((row >> 1) & 7)) << 4;
    const unsigned voA = row * (S_ * 2) + swz;
    auto kappa = [&](int s) {
        const int e = s < 4 - sig ? s : s + sig;
        return e < 16 ? e ^ gj : (e - 12 - sig) ^ gj;
    };
    auto issue_c = [&](int s, int slot) {
        LAS char* sb = smem + slot * P1_SLOT + lw * 1024;
        const int kap = kappa(s);
#pragma unroll
        for (int g = 0; g < 8; ++g) dma16((g & 1) ? ri : rr, sb + g * 2048, voA, kap * 128 + (g >> 1) * (16 * S_ * 2));
    };
    issue_c(0, 2);
    __builtin_amdgcn_sched_barrier(0);
    unsigned voB[5];
#pragma unroll
    for (int b = 0; b < 5; ++b) {
        int c = b * 64 + lane, kr = c / 20, pos = c - kr * 20;
        voB[b] = (kr + 32 * lw) * (D_ * 2) + ((pos ^ (((kr >> 3) & 1) << 1)) << 4);
    }
    auto issue_x = [&](int s, int slot) {
        LAS char* sb = smem + slot * P1_SLOT + P1_AB + lw * 10240;
        const int kap = kappa(s);
#pragma unroll
        for (int b = 0; b < 10; ++b)
            dma16(rx, sb + b * 1024, voB[b % 5], kap * (64 * D_ * 2) + (b / 5) * (16 * D_ * 2));
    };
    issue_x(0, 2);
    issue_c(1, 0); issue_x(1, 0);
    const char* pre = (const char*)wre + et * 80 * (D_ * 2);
    const char* pim = (const char*)wim + et * 80 * (D_ * 2);
    const unsigned voW = row * (D_ * 2) + swz;
    auto issue_w = [&](int u) {
        int sg = u + wrot;
        sg = sg >= 20 ? sg - 20 : sg;
        rsrc_t r = mk_rsrc(sg < 10 ? pre : pim, 80 * D_ * 2);
        const int ks = sg < 10 ? sg : sg - 10;
        LAS char* sb = smem + ((u + 11) & 15) * W_SLOT + lw * 1024;
#pragma unroll
        for (int j = 0; j < 5; ++j) dma16(r, sb + j * 2048, voW, j * 16 * (D_ * 2) + ks * 128);
    };
    int slot = 1, nw = 0, un = 0;
#pragma unroll 1
    for (int s = 0; s < 14; ++s) {
        if (nw == 0) wait_vm<18>(); else if (nw == 1) wait_vm<23>(); else wait_vm<28>();
        wg_barrier();
        issue_c(s + 2, slot); issue_x(s + 2, slot);
        slot = (slot == 2) ? 0 : slot + 1;
        const int e = s + 2 + (sig == 3 ? 2 : sig);
        nw = (e & 3) || un == 5 ? 0 : e == 12 ? 1 : 2;
#pragma unroll 1
        for (int i = 0; i < nw; ++i) issue_w(un++);
    }
    wait_vm<18>(); wg_barrier();
#pragma unroll 1
    for (int u = 5; u < 8; ++u) issue_w(u);
    wait_vm<15>(); wg_barrier();
#pragma unroll 1
    for (int u = 8; u < 12; ++u) issue_w(u);
    wg_barrier();
#pragma unroll 1
    for (int u = 12; u < 18; ++u) issue_w(u);
    wg_barrier();
    wait_vm<0>(); wg_barrier();
    wg_barrier();
    issue_w(18); issue_w(19);
    wait_vm<0>(); wg_barrier();
    if (blockIdx.x == 0 && lw == 0) {
        const u64 tag = ld_agent(ws + EPOCH_OFF) + 1;
#pragma unroll 1
        for (int spin = 0; spin < SPIN_CAP; ++spin) {
            bool ok = true;
#pragma unroll
            for (int i = 0; i < 4; ++i) ok &= ld_agent(ws + FLAG_OFF + (i * 64 + lane) * 128) == tag;
            if (__all(ok)) break;
            __builtin_amdgcn_s_sleep(4);
        }
        if (lane == 0) st_agent(ws + EPOCH_OFF, tag);
    }
}

struct XF { u32x2 lo, hi; };
struct P1Frags { u32x4 c[4]; XF x[5]; };
__device__ __forceinline__ u32x4 cat(const XF& x) { return u32x4{x.lo[0], x.lo[1], x.hi[0], x.hi[1]}; }

template <int KS> __device__ __forceinline__ void p1_rd(int j, P1Frags& n, unsigned a, unsigned b0, unsigned b1, unsigned b4) {
    constexpr int KO = KS * 10240;
    if (j == 0) RDTR(n.x[0].lo, b0, KO);
    if (j == 1) RDTR(n.x[0].hi, b0, KO + 1280);
    if (j == 2) RD128(n.c[0], a, 0);
    if (j == 3) RD128(n.c[1], a, 2048);
    if (j == 4) RD128(n.c[2], a, 4096);
    if (j == 5) RD128(n.c[3], a, 6144);
    if (j == 6) RDTR(n.x[1].lo, b1, KO + 32);
    if (j == 7) RDTR(n.x[1].hi, b1, KO + 32 + 1280);
    if (j == 8) RDTR(n.x[2].lo, b0, KO + 64);
    if (j == 9) RDTR(n.x[2].hi, b0, KO + 64 + 1280);
    if (j == 10) RDTR(n.x[3].lo, b1, KO + 96);
    if (j == 11) RDTR(n.x[3].hi, b1, KO + 96 + 1280);
    if (j == 12) RDTR(n.x[4].lo, b4, KO);
    if (j == 13) RDTR(n.x[4].hi, b4, KO + 1280);
}
template <int KS, bool RD>
__device__ __forceinline__ void p1_half(f32x4 (&acc)[4][5], const P1Frags& f, P1Frags& n, unsigned a, unsigned b0, unsigned b1, unsigned b4) {
#pragma unroll
    for (int i = 0; i < 5; ++i)
#pragma unroll
        for (int g = 0; g < 4; ++g) {
            u32x4 xx = cat(f.x[i]);
            MFMA(acc[g][i], xx, f.c[g]);
            if (RD) p1_rd<KS>(i * 4 + g, n, a, b0, b1, b4);
        }
}
struct WF { u32x4 w[2][5]; };
template <bool RD, bool LD>
__device__ __forceinline__ void p2_stage(f32x4 (&acc)[5], WF& f, u32x4 (&a)[4], unsigned o0, unsigned o1,
                                         rsrc_t ra, unsigned va, unsigned sa) {
#pragma unroll
    for (int i = 0; i < 4; ++i) {
#pragma unroll
        for (int j = 0; j < 5; ++j) {
            MFMA(acc[j], f.w[i >> 1][j], a[i]);
            if (RD && i == 1) RD128(f.w[0][j], o0, j * 2048);
            if (RD && i == 3) RD128(f.w[1][j], o1, j * 2048);
        }
        if (LD) a[i] = __builtin_amdgcn_raw_buffer_load_b128(ra, va + i * 1024, sa, 16);
    }
}
template <int N> __device__ __forceinline__ void wait_ldsn() { asm volatile("s_waitcnt lgkmcnt(%0)" ::"i"(N) : "memory"); }

__device__ __forceinline__ void compute(LAS char* smem, char* ws, char* y, int tb, int nj, int half, int tb2, int et,
                                        int wrot, int w, int lane
) {
    const u64 epoch = ld_agent(ws + EPOCH_OFF);
    const unsigned base = (unsigned)(size_t)smem;
    const int wm = w >> 1, wn = w & 1;
    const int m = lane & 15, g4 = lane >> 4, q = (lane & 15) >> 2, p = lane & 3;
    const unsigned o0 = base + m * 128 + ((g4 ^ ((m >> 1) & 7)) << 4), o1 = o0 ^ 64;
    rsrc_t rpq = mk_rsrc(ws, PQ_BYTES);
    WF w0, w1;
    {
        const unsigned oa0 = o0 + wm * 8192, oa1 = o1 + wm * 8192;
        const unsigned rowb = base + P1_AB + (8 * g4 + q) * 320 + (p >> 1) * 16 + (p & 1) * 8;
        const int sw = (g4 & 1) * 32;
        const unsigned ob0 = rowb + wn * 192 + sw, ob1 = rowb + wn * 192 - sw, ob4 = rowb + (((4 + wn) * 32) ^ sw);
        f32x4 acc[4][5];
#pragma unroll
        for (int g = 0; g < 4; ++g)
#pragma unroll
            for (int i = 0; i < 5; ++i) acc[g][i] = f32x4{0, 0, 0, 0};
        PIN5(acc[0]); PIN5(acc[1]); PIN5(acc[2]); PIN5(acc[3]);
        P1Frags f0, f1;
        unsigned so = 2 * P1_SLOT;
        if (w == 0) STAMP(1);
        wg_barrier();
        if (w == 0) STAMP(2);
#pragma unroll
        for (int j = 0; j < 14; ++j) p1_rd<0>(j, f0, oa0 + so, ob0 + so, ob1 + so, ob4 + so);
#pragma unroll 1
        for (int s = 0; s < P1_STEPS - 1; ++s) {
            wait_lds();
            p1_half<1, true>(acc, f0, f1, oa1 + so, ob0 + so, ob1 + so, ob4 + so);
            so = (so == 2 * P1_SLOT) ? 0 : so + P1_SLOT;
            wg_barrier();
            p1_half<0, true>(acc, f1, f0, oa0 + so, ob0 + so, ob1 + so, ob4 + so);
        }
        wait_lds();
        p1_half<1, true>(acc, f0, f1, oa1 + so, ob0 + so, ob1 + so, ob4 + so);
        wait_lds();
        {
            const unsigned u0 = o0 + 11 * W_SLOT, u1 = o1 + 11 * W_SLOT;
#pragma unroll
            for (int i = 0; i < 5; ++i)
#pragma unroll
                for (int g = 0; g < 4; ++g) {
                    u32x4 xx = cat(f1.x[i]);
                    MFMA(acc[g][i], xx, f1.c[g]);
                    if (g == 0) RD128(w0.w[0][i], u0, i * 2048);
                    if (g == 1) RD128(w0.w[1][i], u1, i * 2048);
                    if (g == 2) RD128(w1.w[0][i], u0, W_SLOT + i * 2048);
                    if (g == 3) RD128(w1.w[1][i], u1, W_SLOT + i * 2048);
                }
        }
        asm volatile("s_nop 7\n\ts_nop 7" : "+v"(acc[0][0]), "+v"(acc[0][1]), "+v"(acc[0][2]), "+v"(acc[0][3]), "+v"(acc[0][4]),
                     "+v"(acc[1][0]), "+v"(acc[1][1]), "+v"(acc[1][2]), "+v"(acc[1][3]), "+v"(acc[1][4]),
                     "+v"(acc[2][0]), "+v"(acc[2][1]), "+v"(acc[2][2]), "+v"(acc[2][3]), "+v"(acc[2][4]),
                     "+v"(acc[3][0]), "+v"(acc[3][1]), "+v"(acc[3][2]), "+v"(acc[3][3]), "+v"(acc[3][4]));
        wait_lds();
        if (w == 0) STAMP(18);
        const unsigned sb = (((4 * tb + 2 * wm) * 40 + nj * 5) * 2 + half) * 1024;
        const unsigned v16 = wn * 6144 + lane * 16, v8 = 4096 + wn * 512 + (g4 >> 1) * 256 + m * 16 + (g4 & 1) * 8;
#pragma unroll
        for (int g = 0; g < 4; ++g) {
            const unsigned sg = (g & 1) ? 0x80008000u : 0u;
            const unsigned so2 = sb + ((g >> 1) * 40 + (g & 1) * 20) * 2048;
            u32x4 v0 = pack_pair(acc[g][0], acc[g][1]) ^ u32x4{sg, sg, sg, sg};
            u32x4 v1 = pack_pair(acc[g][2], acc[g][3]) ^ u32x4{sg, sg, sg, sg};
            u32x2 v2 = {pack2(acc[g][4][0], acc[g][4][1]) ^ sg, pack2(acc[g][4][2], acc[g][4][3]) ^ sg};
            __builtin_amdgcn_raw_buffer_store_b128(v0, rpq, v16, so2, 16);
            __builtin_amdgcn_raw_buffer_store_b128(v1, rpq, v16 + 2048, so2, 16);
            __builtin_amdgcn_raw_buffer_store_b64(v2, rpq, v8, so2, 16);
        }
        wg_barrier();
        wait_vm<0>();
        wg_barrier();
        if (w == 0) STAMP(19);
    }
    if (w == 0) {
        const u64 tag = epoch + 1;
        if (lane == 0) st_agent(ws + FLAG_OFF + (tb * 8 + half * 4 + nj) * 128, tag);
        const char* fp = ws + FLAG_OFF + (tb2 * 8 + (lane & 7)) * 128;
#pragma unroll 1
        for (int spin = 0; spin < SPIN_CAP; ++spin) {
            if (__all(ld_agent(fp) == tag)) break;
            __builtin_amdgcn_s_sleep(1);
        }
        STAMP(20);
    }
    f32x4 acc[5];
#pragma unroll
    for (int j = 0; j < 5; ++j) acc[j] = f32x4{0, 0, 0, 0};
    PIN5(acc);
    wg_barrier();
    if (w == 0) STAMP(21);
    const int tg = tb2 * 4 + w;
    const unsigned va = lane * 16, sa = tg * 81920;
    unsigned ao = wrot * 4096;
    auto anext = [&]() { unsigned r = sa + ao; ao = (ao == 19 * 4096) ? 0 : ao + 4096; return r; };
    u32x4 a0[4], a1[4], a2[4], a3[4];
    { unsigned r = anext();
#pragma unroll
      for (int i = 0; i < 4; ++i) a0[i] = __builtin_amdgcn_raw_buffer_load_b128(rpq, va + i * 1024, r, 16); }
    { unsigned r = anext();
#pragma unroll
      for (int i = 0; i < 4; ++i) a1[i] = __builtin_amdgcn_raw_buffer_load_b128(rpq, va + i * 1024, r, 16); }
    { unsigned r = anext();
#pragma unroll
      for (int i = 0; i < 4; ++i) a2[i] = __builtin_amdgcn_raw_buffer_load_b128(rpq, va + i * 1024, r, 16); }
    { unsigned r = anext();
#pragma unroll
      for (int i = 0; i < 4; ++i) a3[i] = __builtin_amdgcn_raw_buffer_load_b128(rpq, va + i * 1024, r, 16); }
    unsigned wo = 13 * W_SLOT;
    auto nxt = [&]() { unsigned r = wo; wo = (wo == 15 * W_SLOT) ? 0 : wo + W_SLOT; return r; };
#pragma unroll 1
    for (int it = 0; it < 4; ++it) {
        unsigned r;
        if (it == 1) wg_barrier();
        wait_ldsn<10>(); r = nxt(); p2_stage<true, true>(acc, w0, a0, o0 + r, o1 + r, rpq, va, anext());
        wait_ldsn<10>(); r = nxt(); p2_stage<true, true>(acc, w1, a1, o0 + r, o1 + r, rpq, va, anext());
        wait_ldsn<10>(); r = nxt(); p2_stage<true, true>(acc, w0, a2, o0 + r, o1 + r, rpq, va, anext());
        wait_ldsn<10>(); r = nxt(); p2_stage<true, true>(acc, w1, a3, o0 + r, o1 + r, rpq, va, anext());
    }
    wg_barrier();
    p2_stage<true, false>(acc, w0, a0, o0 + 13 * W_SLOT, o1 + 13 * W_SLOT, rpq, va, 0);
    p2_stage<true, false>(acc, w1, a1, o0 + 14 * W_SLOT, o1 + 14 * W_SLOT, rpq, va, 0);
    wait_ldsn<10>(); p2_stage<false, false>(acc, w0, a2, 0, 0, rpq, va, 0);
    wait_ldsn<0>(); p2_stage<false, false>(acc, w1, a3, 0, 0, rpq, va, 0);
    asm volatile("s_nop 7\n\ts_nop 7" : "+v"(acc[0]), "+v"(acc[1]), "+v"(acc[2]), "+v"(acc[3]), "+v"(acc[4]));
    if (w == 0) STAMP(26);
    rsrc_t ry = mk_rsrc(y, S_ * D_ * 2);
    const unsigned vy = m * (D_ * 2) + g4 * 16, sy = tg * (16 * D_ * 2) + et * 160;
    __builtin_amdgcn_raw_buffer_store_b128(pack_pair(acc[0], acc[1]), ry, vy, sy, YAUX);
    __builtin_amdgcn_raw_buffer_store_b128(pack_pair(acc[2], acc[3]), ry, vy + 64, sy, YAUX);
    __builtin_amdgcn_raw_buffer_store_b64(u32x2{pack2(acc[4][0], acc[4][1]), pack2(acc[4][2], acc[4][3])}, ry,
                                          m * (D_ * 2) + 128 + g4 * 8, sy, YAUX);
}
#endif

__global__ __launch_bounds__(384, 2) void fnet_fused(const void* __restrict__ x, const void* __restrict__ cr,
                                                     const void* __restrict__ ci, const void* __restrict__ wre,
                                                     const void* __restrict__ wim, char* ws, char* __restrict__ y) {
#if defined(__HIP_DEVICE_COMPILE__)
    __shared__ __attribute__((aligned(1024))) char smem_[163840];
    LAS char* smem = (LAS char*)smem_;
    u64 pc;
    asm volatile("s_getpc_b64 %0" : "=s"(pc));
    const int bid = blockIdx.x, xcd = bid & 7, idx = bid >> 3;
    const int half = xcd & 1, tbl = idx >> 2, nj = idx & 3, tb = 8 * (xcd >> 1) + tbl;
    const int tb2 = 4 * xcd + (idx >> 3), et = idx & 7;
    const int lane = threadIdx.x & 63;
    const int wid = __builtin_amdgcn_readfirstlane(threadIdx.x >> 6);
    const int sig = STAG ? tbl >> 1 : 0, gj = ((tbl & 1) | (~tbl & 2)) ^ nj;
    const int wrot = 5 * (idx >> 3);
    if (__builtin_expect(wid >= 4, 1)) {
        loader(smem, x, cr, ci, wre, wim, ws, tb, nj, half, sig, gj, et, wrot, wid - 4, lane);
    } else {
        if (wid == 3) {
            pc &= ~255ull;
            unsigned v0, v1, o0 = lane * 128, o1 = (lane < PFCODE - 64 ? lane + 64 : lane) * 128;
            asm volatile("global_load_dword %0, %2, %4\n\tglobal_load_dword %1, %3, %4\n\ts_waitcnt vmcnt(0)"
                         : "=&v"(v0), "=&v"(v1) : "v"(o0), "v"(o1), "s"(pc) : "memory");
        }
        compute(smem, ws, y, tb, nj, half, tb2, et, wrot, wid, lane);
    }
#endif
}

extern "C" void kernel_launch(void* const* d_in, const int* in_sizes, int n_in, void* d_out, int out_size,
                              void* d_ws, size_t ws_size, hipStream_t stream) {
    (void)in_sizes; (void)n_in; (void)out_size; (void)ws_size;
    fnet_fused<<<256, 384, 0, stream>>>(d_in[0], d_in[1], d_in[2], d_in[3], d_in[4], (char*)d_ws, (char*)d_out);
}
```

```cpp
#include <hip/hip_runtime.h>
#include <hip/hip_bf16.h>

typedef __attribute__((ext_vector_type(2))) __bf16 bf16x2;
typedef __attribute__((ext_vector_type(2))) float f32x2;
typedef __attribute__((ext_vector_type(4))) float f32x4;
typedef __attribute__((ext_vector_type(4))) unsigned u32x4;
typedef __attribute__((ext_vector_type(2))) unsigned u32x2;
typedef unsigned long long u64;
#define LAS __attribute__((address_space(3)))
#define GAS __attribute__((address_space(1)))

constexpr int S_ = 2048, D_ = 640;
constexpr int TGS = 40 * 1024 + 128;
constexpr int PQ_HALF = 128 * TGS;
constexpr int PQ_BYTES = 2 * PQ_HALF;
constexpr int FLAG_OFF = PQ_BYTES;
constexpr int EPOCH_OFF = FLAG_OFF + 256 * 128;
#define RIDE 0x111
#define W16EARLY 1
#define TOUCH 0
#define NA 12
#define PFCODE 140
#define YAUX 0
#define STAMP(i)

#if defined(__HIP_DEVICE_COMPILE__)
typedef __amdgpu_buffer_rsrc_t rsrc_t;
__device__ __forceinline__ u64 rt() { u64 t; asm volatile("s_memrealtime %0\n\ts_waitcnt lgkmcnt(0)" : "=s"(t) :: "memory"); return t; }
template <int N> __device__ __forceinline__ void wait_vm() { asm volatile("s_waitcnt vmcnt(%0)" ::"i"(N) : "memory"); }
__device__ __forceinline__ void wait_lds() { asm volatile("s_waitcnt lgkmcnt(0)" ::: "memory"); }
__device__ __forceinline__ void wg_barrier() { asm volatile("s_waitcnt lgkmcnt(0)\n\ts_barrier" ::: "memory"); }
__device__ __forceinline__ rsrc_t mk_rsrc(const void* p, unsigned bytes) {
    return __builtin_amdgcn_make_buffer_rsrc((void*)p, 0, bytes, 0x00020000);
}
__device__ __forceinline__ void dma16(rsrc_t r, LAS char* dst, unsigned voff, unsigned soff) {
    __builtin_amdgcn_raw_ptr_buffer_load_lds(r, (LAS void*)dst, 16, voff, soff, 0, 0);
}
__device__ __forceinline__ unsigned pack2(float a, float b) {
    f32x2 v = {a, b};
    return __builtin_bit_cast(unsigned, __builtin_convertvector(v, bf16x2));
}
__device__ __forceinline__ u32x4 pack_pair(f32x4 a, f32x4 b) {
    unsigned a0 = pack2(a[0], a[1]), a1 = pack2(a[2], a[3]);
    unsigned b0 = pack2(b[0], b[1]), b1 = pack2(b[2], b[3]);
    auto s0 = __builtin_amdgcn_permlane32_swap(a0, b0, false, false);
    auto s1 = __builtin_amdgcn_permlane32_swap(a1, b1, false, false);
    auto t0 = __builtin_amdgcn_permlane16_swap(s0[0], s0[1], false, false);
    auto t1 = __builtin_amdgcn_permlane16_swap(s1[0], s1[1], false, false);
    u32x4 r = {t0[0], t1[0], t0[1], t1[1]};
    return r;
}
#define PIN5(a) asm volatile("" : "+v"(a[0]), "+v"(a[1]), "+v"(a[2]), "+v"(a[3]), "+v"(a[4]))
#define MFMA(acc, a, b) asm volatile("v_mfma_f32_16x16x32_bf16 %0, %1, %2, %0" : "+v"(acc) : "v"(a), "v"(b))
#define RD128(d, ad, off) asm volatile("ds_read_b128 %0, %1 offset:%2" : "=v"(d) : "v"(ad), "i"(off))
#define RE128(d, ad, off) asm volatile("ds_read_b128 %0, %1 offset:%2" : "+v"(d) : "v"(ad), "i"(off))
#define RDTR(d, ad, off) asm volatile("ds_read_b64_tr_b16 %0, %1 offset:%2" : "=v"(d) : "v"(ad), "i"(off))
__device__ __forceinline__ u64 ld_agent(const char* p) {
    return __hip_atomic_load((GAS u64*)p, __ATOMIC_RELAXED, __HIP_MEMORY_SCOPE_AGENT);
}
__device__ __forceinline__ void st_agent(char* p, u64 v) {
    __hip_atomic_store((GAS u64*)p, v, __ATOMIC_RELAXED, __HIP_MEMORY_SCOPE_AGENT);
}

constexpr int P1_AB = 16384, P1_SLOT = 36864, NB = 32 - NA;
constexpr int W_SLOT = 10240;
constexpr int SPIN_CAP = 1 << 20;

__device__ __forceinline__ void poll(const char* p, u64 tag, int nap) {
#pragma unroll 1
    for (int spin = 0; spin < SPIN_CAP; ++spin) {
        if (__all(ld_agent(p) == tag)) break;
        if (nap) __builtin_amdgcn_s_sleep(4); else __builtin_amdgcn_s_sleep(1);
    }
    asm volatile("" ::: "memory");
}

__device__ __forceinline__ void loader(LAS char* smem, const void* x, const void* cr, const void* ci, const void* wre,
                                       const void* wim, char* ws, int tb, int nj, int hi, int lng, int gj, int et,
                                       int wrot, int lw, int lane) {
    rsrc_t rr = mk_rsrc((const char*)cr + (size_t)tb * (64 * S_ * 2), 64 * S_ * 2);
    rsrc_t ri = mk_rsrc((const char*)ci + (size_t)tb * (64 * S_ * 2), 64 * S_ * 2);
    rsrc_t rx = mk_rsrc((const char*)x + nj * 320, S_ * D_ * 2 - nj * 320);
    const int row = 8 * lw + (lane >> 3);
    const unsigned swz = ((lane & 7) ^ ((row >> 1) & 7)) << 4;
    const unsigned voA = row * (S_ * 2) + swz;
    const int n = lng ? NB : NA;
    const int rot = (((gj + nj) & 3) * (n - 12)) >> 2, flip = hi ? 31 : 0;
    gj ^= nj;
    auto kappa = [&](int s) { return (s < 12 ? s ^ gj : s + rot < n ? s + rot : s + rot + 12 - n) ^ flip; };
    int slot = (96 - n) % 3;
    auto issue_c = [&](int s) {
        LAS char* sb = smem + slot * P1_SLOT + lw * 1024;
        const int kap = kappa(s);
#pragma unroll
        for (int g = 0; g < 8; ++g) dma16((g & 1) ? ri : rr, sb + g * 2048, voA, kap * 128 + (g >> 1) * (16 * S_ * 2));
    };
    issue_c(0);
    __builtin_amdgcn_sched_barrier(0);
    unsigned voB[5];
#pragma unroll
    for (int b = 0; b < 5; ++b) {
        int c = b * 64 + lane, kr = c / 20, pos = c - kr * 20;
        voB[b] = (kr + 32 * lw) * (D_ * 2) + ((pos ^ (((kr >> 3) & 1) << 1)) << 4);
    }
    auto issue_x = [&](int s) {
        LAS char* sb = smem + slot * P1_SLOT + P1_AB + lw * 10240;
        const int kap = kappa(s);
#pragma unroll
        for (int b = 0; b < 10; ++b)
            dma16(rx, sb + b * 1024, voB[b % 5], kap * (64 * D_ * 2) + (b / 5) * (16 * D_ * 2));
        slot = (slot == 2) ? 0 : slot + 1;
    };
    issue_x(0);
    issue_c(1); issue_x(1);
    const u64 tag = ld_agent(ws + EPOCH_OFF) + 1;
    const char* pre = (const char*)wre + et * 80 * (D_ * 2);
    const char* pim = (const char*)wim + et * 80 * (D_ * 2);
    const unsigned voW = row * (D_ * 2) + swz;
    auto issue_w = [&](int u0, int u1) {
#pragma unroll 1
        for (int u = u0; u < u1; ++u) {
            int sg = u + wrot;
            sg = sg >= 20 ? sg - 20 : sg;
            rsrc_t r = mk_rsrc(sg < 10 ? pre : pim, 80 * D_ * 2);
            const int ks = sg < 10 ? sg : sg - 10;
            LAS char* sb = smem + ((u + 11) & 15) * W_SLOT + lw * 1024;
#pragma unroll
            for (int j = 0; j < 5; ++j) dma16(r, sb + j * 2048, voW, j * 16 * (D_ * 2) + ks * 128);
        }
    };
    int nw = 0, un = 0;
#pragma unroll 1
    for (int s = 0; s < n - 2; ++s) {
        if (nw == 0) wait_vm<18>(); else if (nw == 1) wait_vm<23>(); else wait_vm<28>();
        wg_barrier();
        issue_c(s + 2); issue_x(s + 2);
        nw = (!lng && ((RIDE >> s) & 1)) ? (un == 4 ? 1 : 2) : 0;
        issue_w(un, un + nw);
        un += nw;
    }
    if (lng) {
        const char* pf = ws + FLAG_OFF + (tb * 8 + (hi ^ 1) * 4 + nj) * 128;
        const u64 seen = ld_agent(pf);
        wait_vm<19>(); wg_barrier();
        if (lw == 0 && !__all(seen == tag)) poll(pf, tag, 0);
        issue_w(0, 3);
        wait_vm<15>(); wg_barrier();
        __builtin_amdgcn_s_sleep(2);
        issue_w(3, 6);
        wait_vm<15>(); wg_barrier();
        issue_w(6, 16);
        wg_barrier();
    } else {
        wait_vm<18>(); wg_barrier();
        wait_vm<0>(); wg_barrier();
        wg_barrier();
        wg_barrier();
        __builtin_amdgcn_s_sleep(2);
        issue_w(5, 16);
    }
    __builtin_amdgcn_s_sleep(4);
    issue_w(16, 18);
    wait_vm<0>(); wg_barrier();
    wg_barrier();
    issue_w(W16EARLY ? 18 : 16, 20);
    wait_vm<0>(); wg_barrier();
    if (blockIdx.x == 0 && lw == 0) {
#pragma unroll 1
        for (int i = 0; i < 4; ++i) poll(ws + FLAG_OFF + (i * 64 + lane) * 128, tag, 1);
        if (lane == 0) st_agent(ws + EPOCH_OFF, tag);
    }
}

struct XF { u32x2 lo, hi; };
struct P1Frags { u32x4 c[4]; XF x[5]; };
__device__ __forceinline__ u32x4 cat(const XF& x) { return u32x4{x.lo[0], x.lo[1], x.hi[0], x.hi[1]}; }

template <int KS> __device__ __forceinline__ void p1_rd(int j, P1Frags& n, unsigned a, unsigned b0, unsigned b1, unsigned b4) {
    constexpr int KO = KS * 10240;
    if (j == 0) RDTR(n.x[0].lo, b0, KO);
    if (j == 1) RDTR(n.x[0].hi, b0, KO + 1280);
    if (j == 2) RD128(n.c[0], a, 0);
    if (j == 3) RD128(n.c[1], a, 2048);
    if (j == 4) RD128(n.c[2], a, 4096);
    if (j == 5) RD128(n.c[3], a, 6144);
    if (j == 6) RDTR(n.x[1].lo, b1, KO + 32);
    if (j == 7) RDTR(n.x[1].hi, b1, KO + 32 + 1280);
    if (j == 8) RDTR(n.x[2].lo, b0, KO + 64);
    if (j == 9) RDTR(n.x[2].hi, b0, KO + 64 + 1280);
    if (j == 10) RDTR(n.x[3].lo, b1, KO + 96);
    if (j == 11) RDTR(n.x[3].hi, b1, KO + 96 + 1280);
    if (j == 12) RDTR(n.x[4].lo, b4, KO);
    if (j == 13) RDTR(n.x[4].hi, b4, KO + 1280);
}
template <int KS, bool RD>
__device__ __forceinline__ void p1_half(f32x4 (&acc)[4][5], const P1Frags& f, P1Frags& n, unsigned a, unsigned b0, unsigned b1, unsigned b4) {
#pragma unroll
    for (int i = 0; i < 5; ++i)
#pragma unroll
        for (int g = 0; g < 4; ++g) {
            u32x4 xx = cat(f.x[i]);
            MFMA(acc[g][i], xx, f.c[g]);
            if (RD) p1_rd<KS>(i * 4 + g, n, a, b0, b1, b4);
        }
}
struct WF { u32x4 w[2][5]; };
template <bool RD>
__device__ __forceinline__ void p2_visit(f32x4 (&acc)[5], WF& f, const u32x4& a0, const u32x4& a1, unsigned o0, unsigned o1) {
#pragma unroll
    for (int i = 0; i < 2; ++i)
#pragma unroll
        for (int j = 0; j < 5; ++j) {
            MFMA(acc[j], f.w[i][j], (i ? a1 : a0));
            if (RD && i == 0) RD128(f.w[0][j], o0, j * 2048);
            if (RD && i == 1) RD128(f.w[1][j], o1, j * 2048);
        }
}
template <int N> __device__ __forceinline__ void wait_ldsn() { asm volatile("s_waitcnt lgkmcnt(%0)" ::"i"(N) : "memory"); }

__device__ __forceinline__ void compute(LAS char* smem, char* ws, char* y, int tb, int nj, int hi, int lng, int tb2,
                                        int et, int wrot, int w, int lane
) {
    const u64 epoch = ld_agent(ws + EPOCH_OFF);
    const unsigned base = (unsigned)(size_t)smem;
    const int wm = w >> 1, wn = w & 1;
    const int m = lane & 15, g4 = lane >> 4, q = (lane & 15) >> 2, p = lane & 3;
    const unsigned o0 = base + m * 128 + ((g4 ^ ((m >> 1) & 7)) << 4), o1 = o0 ^ 64;
    rsrc_t rpq = mk_rsrc(ws, PQ_BYTES);
    WF w0, w1;
    {
        const unsigned oa0 = o0 + wm * 8192, oa1 = o1 + wm * 8192;
        const unsigned rowb = base + P1_AB + (8 * g4 + q) * 320 + (p >> 1) * 16 + (p & 1) * 8;
        const int sw = (g4 & 1) * 32;
        const unsigned ob0 = rowb + wn * 192 + sw, ob1 = rowb + wn * 192 - sw, ob4 = rowb + (((4 + wn) * 32) ^ sw);
        f32x4 acc[4][5];
#pragma unroll
        for (int g = 0; g < 4; ++g)
#pragma unroll
            for (int i = 0; i < 5; ++i) acc[g][i] = f32x4{0, 0, 0, 0};
        PIN5(acc[0]); PIN5(acc[1]); PIN5(acc[2]); PIN5(acc[3]);
        u32x4 id[2], idw;
        {
            const unsigned one = (m & 1) ? 0x3f800000u : 0x3f80u;
#pragma unroll
            for (int r = 0; r < 4; ++r) {
                const int k2 = 4 * g4 + r;
                id[0][r] = k2 == (m >> 1) ? one : 0u;
                id[1][r] = k2 == (m >> 1) + 8 ? one : 0u;
                idw[r] = k2 == (m >> 1) + 8 * wn ? one : 0u;
            }
            asm volatile("" : "+v"(id[0]), "+v"(id[1]), "+v"(idw));
        }
        P1Frags f0, f1;
        const int n = lng ? NB : NA;
        unsigned so = ((96 - n) % 3) * P1_SLOT;
        if (w == 0) STAMP(1);
        wg_barrier();
        if (w == 0) STAMP(2);
#pragma unroll
        for (int j = 0; j < 14; ++j) p1_rd<0>(j, f0, oa0 + so, ob0 + so, ob1 + so, ob4 + so);
#pragma unroll 1
        for (int s = 0; s < n - 2; ++s) {
            wait_lds();
            p1_half<1, true>(acc, f0, f1, oa1 + so, ob0 + so, ob1 + so, ob4 + so);
            so = (so == 2 * P1_SLOT) ? 0 : so + P1_SLOT;
            wg_barrier();
            p1_half<0, true>(acc, f1, f0, oa0 + so, ob0 + so, ob1 + so, ob4 + so);
        }
        wait_lds();
        p1_half<1, true>(acc, f0, f1, oa1 + so, ob0 + so, ob1 + so, ob4 + so);
        so = 2 * P1_SLOT;
        wg_barrier();
        if (w == 0) STAMP(17);
        const unsigned sb = (4 * tb + 2 * wm) * TGS + nj * 5 * 1024;
        const unsigned u0 = o0 + 11 * W_SLOT, u1 = o1 + 11 * W_SLOT;
        u32x4 pt[4][3];
        {
            rsrc_t rpt = mk_rsrc(ws, lng ? PQ_BYTES : 0);
#pragma unroll
            for (int g = 0; g < 4; ++g) {
                const unsigned so2 = PQ_HALF + sb + (g >> 1) * TGS + (g & 1) * 20 * 1024;
                pt[g][0] = __builtin_amdgcn_raw_buffer_load_b128(rpt, wn * 3072 + lane * 16, so2, 16);
                pt[g][1] = __builtin_amdgcn_raw_buffer_load_b128(rpt, wn * 3072 + 1024 + lane * 16, so2, 16);
                pt[g][2] = __builtin_amdgcn_raw_buffer_load_b128(rpt, 2048 + lane * 16, so2, 16);
            }
        }
        p1_half<0, true>(acc, f1, f0, oa0 + so, ob0 + so, ob1 + so, ob4 + so);
        wait_lds();
        p1_half<1, true>(acc, f0, f1, oa1 + so, ob0 + so, ob1 + so, ob4 + so);
        wait_lds();
        p1_half<0, false>(acc, f1, f0, 0, 0, 0, 0);
        if (lng) {
#pragma unroll
            for (int g = 0; g < 4; ++g)
#pragma unroll
                for (int i = 0; i < 5; ++i) {
                    MFMA(acc[g][i], (i == 4 ? idw : id[i & 1]), pt[g][i >> 1]);
                }
        }
        asm volatile("s_nop 7\n\ts_nop 7" : "+v"(acc[0][0]), "+v"(acc[0][1]), "+v"(acc[0][2]), "+v"(acc[0][3]), "+v"(acc[0][4]),
                     "+v"(acc[1][0]), "+v"(acc[1][1]), "+v"(acc[1][2]), "+v"(acc[1][3]), "+v"(acc[1][4]),
                     "+v"(acc[2][0]), "+v"(acc[2][1]), "+v"(acc[2][2]), "+v"(acc[2][3]), "+v"(acc[2][4]),
                     "+v"(acc[3][0]), "+v"(acc[3][1]), "+v"(acc[3][2]), "+v"(acc[3][3]), "+v"(acc[3][4]));
        wait_lds();
        if (w == 0) STAMP(18);
        const unsigned v16 = wn * 3072 + lane * 16, v8 = 2048 + wn * 512 + (g4 >> 1) * 256 + m * 16 + (g4 & 1) * 8;
        const unsigned neg = lng ? 0x80008000u : 0u, dst = sb + (lng ? 0 : PQ_HALF);
#pragma unroll
        for (int g = 0; g < 4; ++g) {
            const unsigned sg = (g & 1) ? neg : 0u;
            const unsigned so2 = dst + (g >> 1) * TGS + (g & 1) * 20 * 1024;
            u32x4 v0 = pack_pair(acc[g][0], acc[g][1]) ^ u32x4{sg, sg, sg, sg};
            u32x4 v1 = pack_pair(acc[g][2], acc[g][3]) ^ u32x4{sg, sg, sg, sg};
            u32x2 v2 = {pack2(acc[g][4][0], acc[g][4][1]) ^ sg, pack2(acc[g][4][2], acc[g][4][3]) ^ sg};
            __builtin_amdgcn_raw_buffer_store_b128(v0, rpq, v16, so2, 16);
            __builtin_amdgcn_raw_buffer_store_b128(v1, rpq, v16 + 1024, so2, 16);
            __builtin_amdgcn_raw_buffer_store_b64(v2, rpq, v8, so2, 16);
        }
        wg_barrier();
#pragma unroll
        for (int i = 0; i < 5; ++i) {
            RD128(w0.w[0][i], u0, i * 2048); RD128(w0.w[1][i], u1, i * 2048);
            RD128(w1.w[0][i], u0, W_SLOT + i * 2048); RD128(w1.w[1][i], u1, W_SLOT + i * 2048);
            if (i == 2) wait_lds();
        }
        wait_vm<0>();
        wg_barrier();
        if (w == 0 && lane == 0) st_agent(ws + FLAG_OFF + (tb * 8 + hi * 4 + nj) * 128, epoch + 1);
        if (w == 0) STAMP(19);
    }
    if (w == 0) {
        poll(ws + FLAG_OFF + (tb2 * 8 + ((tb2 & 1) ^ 1) * 4 + (lane & 3)) * 128, epoch + 1, 0);
        STAMP(20);
    }
    f32x4 acc[5];
#pragma unroll
    for (int j = 0; j < 5; ++j) acc[j] = f32x4{0, 0, 0, 0};
    PIN5(acc);
    wg_barrier();
    if (w == 0) STAMP(21);
    const int tg = tb2 * 4 + w;
    const unsigned va = lane * 16;
    unsigned sab[4];
#pragma unroll
    for (int b = 0; b < 4; ++b) sab[b] = tg * TGS + ((wrot + 5 * b) % 20) * 2048;
    constexpr int AD = 10;
    u32x4 a[2 * AD];
    auto lda = [&](int u) {
        const unsigned sa = sab[u / 5] + (u % 5) * 2048;
        a[2 * (u % AD)] = __builtin_amdgcn_raw_buffer_load_b128(rpq, va, sa, 16);
        a[2 * (u % AD) + 1] = __builtin_amdgcn_raw_buffer_load_b128(rpq, va + 1024, sa, 16);
    };
#pragma unroll
    for (int u = 0; u < AD; ++u) lda(u);
#pragma unroll
    for (int u = 0; u < 20; ++u) {
        if (u == 4 || u == (W16EARLY ? 16 : 14)) wg_barrier();
        if (u < 19) wait_ldsn<10>(); else wait_ldsn<0>();
        const unsigned r = ((u + 13) & 15) * W_SLOT;
        if (u < 18) p2_visit<true>(acc, (u & 1) ? w1 : w0, a[2 * (u % AD)], a[2 * (u % AD) + 1], o0 + r, o1 + r);
        else p2_visit<false>(acc, (u & 1) ? w1 : w0, a[2 * (u % AD)], a[2 * (u % AD) + 1], 0, 0);
        if (u + AD < 20) lda(u + AD);
    }
    asm volatile("s_nop 7\n\ts_nop 7" : "+v"(acc[0]), "+v"(acc[1]), "+v"(acc[2]), "+v"(acc[3]), "+v"(acc[4]));
    if (w == 0) STAMP(26);
    rsrc_t ry = mk_rsrc(y, S_ * D_ * 2);
    const unsigned vy = m * (D_ * 2) + g4 * 16, sy = tg * (16 * D_ * 2) + et * 160;
    __builtin_amdgcn_raw_buffer_store_b128(pack_pair(acc[0], acc[1]), ry, vy, sy, YAUX);
    __builtin_amdgcn_raw_buffer_store_b128(pack_pair(acc[2], acc[3]), ry, vy + 64, sy, YAUX);
    __builtin_amdgcn_raw_buffer_store_b64(u32x2{pack2(acc[4][0], acc[4][1]), pack2(acc[4][2], acc[4][3])}, ry,
                                          m * (D_ * 2) + 128 + g4 * 8, sy, YAUX);
}
#endif

__global__ __launch_bounds__(384, 2) void fnet_fused(const void* __restrict__ x, const void* __restrict__ cr,
                                                     const void* __restrict__ ci, const void* __restrict__ wre,
                                                     const void* __restrict__ wim, char* ws, char* __restrict__ y) {
#if defined(__HIP_DEVICE_COMPILE__)
    __shared__ __attribute__((aligned(1024))) char smem_[163840];
    LAS char* smem = (LAS char*)smem_;
    u64 pc;
    asm volatile("s_getpc_b64 %0" : "=s"(pc));
    const int bid = blockIdx.x, xcd = bid & 7, idx = bid >> 3;
    const int hi = xcd & 1, tbl = idx >> 2, nj = idx & 3, tb = 8 * (xcd >> 1) + tbl, lng = hi ^ (tbl & 1);
    const int tb2 = tb, et = hi * 4 + nj;
    const int lane = threadIdx.x & 63;
    const int wid = __builtin_amdgcn_readfirstlane(threadIdx.x >> 6);
    const int gj = tbl >> 1, wrot = (tbl >> 1) * 5;
    if (__builtin_expect(wid >= 4, 1)) {
        loader(smem, x, cr, ci, wre, wim, ws, tb, nj, hi, lng, gj, et, wrot, wid - 4, lane);
    } else {
        if (wid == 3) {
            pc &= ~255ull;
            unsigned v0, v1, v2, o0 = lane * 128, o1 = o0 + 8192, o2 = (lane < PFCODE - 128 ? lane + 128 : lane) * 128;
            asm volatile("global_load_dword %0, %3, %6\n\tglobal_load_dword %1, %4, %6\n\tglobal_load_dword %2, %5, %6\n\ts_waitcnt vmcnt(0)"
                         : "=&v"(v0), "=&v"(v1), "=&v"(v2) : "v"(o0), "v"(o1), "v"(o2), "s"(pc) : "memory");
        }
        compute(smem, ws, y, tb, nj, hi, lng, tb2, et, wrot, wid, lane);
    }
#endif
}

extern "C" void kernel_launch(void* const* d_in, const int* in_sizes, int n_in, void* d_out, int out_size,
                              void* d_ws, size_t ws_size, hipStream_t stream) {
    (void)in_sizes; (void)n_in; (void)out_size; (void)ws_size;
    fnet_fused<<<256, 384, 0, stream>>>(d_in[0], d_in[1], d_in[2], d_in[3], d_in[4], (char*)d_ws, (char*)d_out);
}
```

```cpp
#define AMPT 33
#include <hip/hip_runtime.h>
#include <hip/hip_bf16.h>

typedef __attribute__((ext_vector_type(2))) __bf16 bf16x2;
typedef __attribute__((ext_vector_type(2))) float f32x2;
typedef __attribute__((ext_vector_type(4))) float f32x4;
typedef __attribute__((ext_vector_type(4))) unsigned u32x4;
typedef __attribute__((ext_vector_type(2))) unsigned u32x2;
typedef unsigned long long u64;
#define LAS __attribute__((address_space(3)))
#define GAS __attribute__((address_space(1)))

constexpr int S_ = 2048, D_ = 640;
constexpr int TGS = 40 * 1024 + 128;
constexpr int PQ_HALF = 128 * TGS;
constexpr int PQ_BYTES = 2 * PQ_HALF;
constexpr int FLAG_OFF = PQ_BYTES;
constexpr int EPOCH_OFF = FLAG_OFF + 256 * 128;
#define RIDE 0x111
#define W16EARLY 1
#define TOUCH 0
#define NA 12
#define PFCODE 140
#define YAUX 0
#define STAMP(i) do { if ((i) == AMPT) tamp = rt(); } while (0)

#if defined(__HIP_DEVICE_COMPILE__)
typedef __amdgpu_buffer_rsrc_t rsrc_t;
__device__ __forceinline__ u64 rt() { u64 t; asm volatile("s_memrealtime %0\n\ts_waitcnt lgkmcnt(0)" : "=s"(t) :: "memory"); return t; }
template <int N> __device__ __forceinline__ void wait_vm() { asm volatile("s_waitcnt vmcnt(%0)" ::"i"(N) : "memory"); }
__device__ __forceinline__ void wait_lds() { asm volatile("s_waitcnt lgkmcnt(0)" ::: "memory"); }
__device__ __forceinline__ void wg_barrier() { asm volatile("s_waitcnt lgkmcnt(0)\n\ts_barrier" ::: "memory"); }
__device__ __forceinline__ rsrc_t mk_rsrc(const void* p, unsigned bytes) {
    return __builtin_amdgcn_make_buffer_rsrc((void*)p, 0, bytes, 0x00020000);
}
__device__ __forceinline__ void dma16(rsrc_t r, LAS char* dst, unsigned voff, unsigned soff) {
    __builtin_amdgcn_raw_ptr_buffer_load_lds(r, (LAS void*)dst, 16, voff, soff, 0, 0);
}
__device__ __forceinline__ unsigned pack2(float a, float b) {
    f32x2 v = {a, b};
    return __builtin_bit_cast(unsigned, __builtin_convertvector(v, bf16x2));
}
__device__ __forceinline__ u32x4 pack_pair(f32x4 a, f32x4 b) {
    unsigned a0 = pack2(a[0], a[1]), a1 = pack2(a[2], a[3]);
    unsigned b0 = pack2(b[0], b[1]), b1 = pack2(b[2], b[3]);
    auto s0 = __builtin_amdgcn_permlane32_swap(a0, b0, false, false);
    auto s1 = __builtin_amdgcn_permlane32_swap(a1, b1, false, false);
    auto t0 = __builtin_amdgcn_permlane16_swap(s0[0], s0[1], false, false);
    auto t1 = __builtin_amdgcn_permlane16_swap(s1[0], s1[1], false, false);
    u32x4 r = {t0[0], t1[0], t0[1], t1[1]};
    return r;
}
#define PIN5(a) asm volatile("" : "+v"(a[0]), "+v"(a[1]), "+v"(a[2]), "+v"(a[3]), "+v"(a[4]))
#define MFMA(acc, a, b) asm volatile("v_mfma_f32_16x16x32_bf16 %0, %1, %2, %0" : "+v"(acc) : "v"(a), "v"(b))
#define RD128(d, ad, off) asm volatile("ds_read_b128 %0, %1 offset:%2" : "=v"(d) : "v"(ad), "i"(off))
#define RE128(d, ad, off) asm volatile("ds_read_b128 %0, %1 offset:%2" : "+v"(d) : "v"(ad), "i"(off))
#define RDTR(d, ad, off) asm volatile("ds_read_b64_tr_b16 %0, %1 offset:%2" : "=v"(d) : "v"(ad), "i"(off))
__device__ __forceinline__ u64 ld_agent(const char* p) {
    return __hip_atomic_load((GAS u64*)p, __ATOMIC_RELAXED, __HIP_MEMORY_SCOPE_AGENT);
}
__device__ __forceinline__ void st_agent(char* p, u64 v) {
    __hip_atomic_store((GAS u64*)p, v, __ATOMIC_RELAXED, __HIP_MEMORY_SCOPE_AGENT);
}

constexpr int P1_AB = 16384, P1_SLOT = 36864, NB = 32 - NA;
constexpr int W_SLOT = 10240;
constexpr int SPIN_CAP = 1 << 20;

__device__ __forceinline__ void poll(const char* p, u64 tag, int nap) {
#pragma unroll 1
    for (int spin = 0; spin < SPIN_CAP; ++spin) {
        if (__all(ld_agent(p) == tag)) break;
        if (nap) __builtin_amdgcn_s_sleep(4); else __builtin_amdgcn_s_sleep(1);
    }
    asm volatile("" ::: "memory");
}

__device__ __forceinline__ void loader(LAS char* smem, const void* x, const void* cr, const void* ci, const void* wre,
                                       const void* wim, char* ws, int tb, int nj, int hi, int lng, int gj, int et,
                                       int wrot, int lw, int lane) {
    rsrc_t rr = mk_rsrc((const char*)cr + (size_t)tb * (64 * S_ * 2), 64 * S_ * 2);
    rsrc_t ri = mk_rsrc((const char*)ci + (size_t)tb * (64 * S_ * 2), 64 * S_ * 2);
    rsrc_t rx = mk_rsrc((const char*)x + nj * 320, S_ * D_ * 2 - nj * 320);
    const int row = 8 * lw + (lane >> 3);
    const unsigned swz = ((lane & 7) ^ ((row >> 1) & 7)) << 4;
    const unsigned voA = row * (S_ * 2) + swz;
    const int n = lng ? NB : NA;
    const int rot = (((gj + nj) & 3) * (n - 12)) >> 2, flip = hi ? 31 : 0;
    gj ^= nj;
    auto kappa = [&](int s) { return (s < 12 ? s ^ gj : s + rot < n ? s + rot : s + rot + 12 - n) ^ flip; };
    int slot = (96 - n) % 3;
    auto issue_c = [&](int s) {
        LAS char* sb = smem + slot * P1_SLOT + lw * 1024;
        const int kap = kappa(s);
#pragma unroll
        for (int g = 0; g < 8; ++g) dma16((g & 1) ? ri : rr, sb + g * 2048, voA, kap * 128 + (g >> 1) * (16 * S_ * 2));
    };
    issue_c(0);
    __builtin_amdgcn_sched_barrier(0);
    unsigned voB[5];
#pragma unroll
    for (int b = 0; b < 5; ++b) {
        int c = b * 64 + lane, kr = c / 20, pos = c - kr * 20;
        voB[b] = (kr + 32 * lw) * (D_ * 2) + ((pos ^ (((kr >> 3) & 1) << 1)) << 4);
    }
    auto issue_x = [&](int s) {
        LAS char* sb = smem + slot * P1_SLOT + P1_AB + lw * 10240;
        const int kap = kappa(s);
#pragma unroll
        for (int b = 0; b < 10; ++b)
            dma16(rx, sb + b * 1024, voB[b % 5], kap * (64 * D_ * 2) + (b / 5) * (16 * D_ * 2));
        slot = (slot == 2) ? 0 : slot + 1;
    };
    issue_x(0);
    issue_c(1); issue_x(1);
    const u64 tag = ld_agent(ws + EPOCH_OFF) + 1;
    const char* pre = (const char*)wre + et * 80 * (D_ * 2);
    const char* pim = (const char*)wim + et * 80 * (D_ * 2);
    const unsigned voW = row * (D_ * 2) + swz;
    auto issue_w = [&](int u0, int u1) {
#pragma unroll 1
        for (int u = u0; u < u1; ++u) {
            int sg = u + wrot;
            sg = sg >= 20 ? sg - 20 : sg;
            rsrc_t r = mk_rsrc(sg < 10 ? pre : pim, 80 * D_ * 2);
            const int ks = sg < 10 ? sg : sg - 10;
            LAS char* sb = smem + ((u + 11) & 15) * W_SLOT + lw * 1024;
#pragma unroll
            for (int j = 0; j < 5; ++j) dma16(r, sb + j * 2048, voW, j * 16 * (D_ * 2) + ks * 128);
        }
    };
    int nw = 0, un = 0;
#pragma unroll 1
    for (int s = 0; s < n - 2; ++s) {
        if (nw == 0) wait_vm<18>(); else if (nw == 1) wait_vm<23>(); else wait_vm<28>();
        wg_barrier();
        issue_c(s + 2); issue_x(s + 2);
        nw = (!lng && ((RIDE >> s) & 1)) ? (un == 4 ? 1 : 2) : 0;
        issue_w(un, un + nw);
        un += nw;
    }
    if (lng) {
        const char* pf = ws + FLAG_OFF + (tb * 8 + (hi ^ 1) * 4 + nj) * 128;
        const u64 seen = ld_agent(pf);
        wait_vm<19>(); wg_barrier();
        if (lw == 0 && !__all(seen == tag)) poll(pf, tag, 0);
        issue_w(0, 3);
        wait_vm<15>(); wg_barrier();
        __builtin_amdgcn_s_sleep(2);
        issue_w(3, 6);
        wait_vm<15>(); wg_barrier();
        issue_w(6, 16);
        wg_barrier();
    } else {
        wait_vm<18>(); wg_barrier();
        wait_vm<0>(); wg_barrier();
        wg_barrier();
        wg_barrier();
        __builtin_amdgcn_s_sleep(2);
        issue_w(5, 16);
    }
    __builtin_amdgcn_s_sleep(4);
    issue_w(16, 18);
    wait_vm<0>(); wg_barrier();
    wg_barrier();
    issue_w(W16EARLY ? 18 : 16, 20);
    wait_vm<0>(); wg_barrier();
    if (blockIdx.x == 0 && lw == 0) {
#pragma unroll 1
        for (int i = 0; i < 4; ++i) poll(ws + FLAG_OFF + (i * 64 + lane) * 128, tag, 1);
        if (lane == 0) st_agent(ws + EPOCH_OFF, tag);
    }
}

struct XF { u32x2 lo, hi; };
struct P1Frags { u32x4 c[4]; XF x[5]; };
__device__ __forceinline__ u32x4 cat(const XF& x) { return u32x4{x.lo[0], x.lo[1], x.hi[0], x.hi[1]}; }

template <int KS> __device__ __forceinline__ void p1_rd(int j, P1Frags& n, unsigned a, unsigned b0, unsigned b1, unsigned b4) {
    constexpr int KO = KS * 10240;
    if (j == 0) RDTR(n.x[0].lo, b0, KO);
    if (j == 1) RDTR(n.x[0].hi, b0, KO + 1280);
    if (j == 2) RD128(n.c[0], a, 0);
    if (j == 3) RD128(n.c[1], a, 2048);
    if (j == 4) RD128(n.c[2], a, 4096);
    if (j == 5) RD128(n.c[3], a, 6144);
    if (j == 6) RDTR(n.x[1].lo, b1, KO + 32);
    if (j == 7) RDTR(n.x[1].hi, b1, KO + 32 + 1280);
    if (j == 8) RDTR(n.x[2].lo, b0, KO + 64);
    if (j == 9) RDTR(n.x[2].hi, b0, KO + 64 + 1280);
    if (j == 10) RDTR(n.x[3].lo, b1, KO + 96);
    if (j == 11) RDTR(n.x[3].hi, b1, KO + 96 + 1280);
    if (j == 12) RDTR(n.x[4].lo, b4, KO);
    if (j == 13) RDTR(n.x[4].hi, b4, KO + 1280);
}
template <int KS, bool RD>
__device__ __forceinline__ void p1_half(f32x4 (&acc)[4][5], const P1Frags& f, P1Frags& n, unsigned a, unsigned b0, unsigned b1, unsigned b4) {
#pragma unroll
    for (int i = 0; i < 5; ++i)
#pragma unroll
        for (int g = 0; g < 4; ++g) {
            u32x4 xx = cat(f.x[i]);
            MFMA(acc[g][i], xx, f.c[g]);
            if (RD) p1_rd<KS>(i * 4 + g, n, a, b0, b1, b4);
        }
}
struct WF { u32x4 w[2][5]; };
template <bool RD>
__device__ __forceinline__ void p2_visit(f32x4 (&acc)[5], WF& f, const u32x4& a0, const u32x4& a1, unsigned o0, unsigned o1) {
#pragma unroll
    for (int i = 0; i < 2; ++i)
#pragma unroll
        for (int j = 0; j < 5; ++j) {
            MFMA(acc[j], f.w[i][j], (i ? a1 : a0));
            if (RD && i == 0) RD128(f.w[0][j], o0, j * 2048);
            if (RD && i == 1) RD128(f.w[1][j], o1, j * 2048);
        }
}
template <int N> __device__ __forceinline__ void wait_ldsn() { asm volatile("s_waitcnt lgkmcnt(%0)" ::"i"(N) : "memory"); }

__device__ __forceinline__ void compute(LAS char* smem, char* ws, char* y, int tb, int nj, int hi, int lng, int tb2,
                                        int et, int wrot, int w, int lane
    , u64 t0
) {
    u64 tamp = t0;
    const u64 epoch = ld_agent(ws + EPOCH_OFF);
    const unsigned base = (unsigned)(size_t)smem;
    const int wm = w >> 1, wn = w & 1;
    const int m = lane & 15, g4 = lane >> 4, q = (lane & 15) >> 2, p = lane & 3;
    const unsigned o0 = base + m * 128 + ((g4 ^ ((m >> 1) & 7)) << 4), o1 = o0 ^ 64;
    rsrc_t rpq = mk_rsrc(ws, PQ_BYTES);
    WF w0, w1;
    {
        const unsigned oa0 = o0 + wm * 8192, oa1 = o1 + wm * 8192;
        const unsigned rowb = base + P1_AB + (8 * g4 + q) * 320 + (p >> 1) * 16 + (p & 1) * 8;
        const int sw = (g4 & 1) * 32;
        const unsigned ob0 = rowb + wn * 192 + sw, ob1 = rowb + wn * 192 - sw, ob4 = rowb + (((4 + wn) * 32) ^ sw);
        f32x4 acc[4][5];
#pragma unroll
        for (int g = 0; g < 4; ++g)
#pragma unroll
            for (int i = 0; i < 5; ++i) acc[g][i] = f32x4{0, 0, 0, 0};
        PIN5(acc[0]); PIN5(acc[1]); PIN5(acc[2]); PIN5(acc[3]);
        u32x4 id[2], idw;
        {
            const unsigned one = (m & 1) ? 0x3f800000u : 0x3f80u;
#pragma unroll
            for (int r = 0; r < 4; ++r) {
                const int k2 = 4 * g4 + r;
                id[0][r] = k2 == (m >> 1) ? one : 0u;
                id[1][r] = k2 == (m >> 1) + 8 ? one : 0u;
                idw[r] = k2 == (m >> 1) + 8 * wn ? one : 0u;
            }
            asm volatile("" : "+v"(id[0]), "+v"(id[1]), "+v"(idw));
        }
        P1Frags f0, f1;
        const int n = lng ? NB : NA;
        unsigned so = ((96 - n) % 3) * P1_SLOT;
        if (w == 0) STAMP(1);
        wg_barrier();
        if (w == 0) STAMP(2);
#pragma unroll
        for (int j = 0; j < 14; ++j) p1_rd<0>(j, f0, oa0 + so, ob0 + so, ob1 + so, ob4 + so);
#pragma unroll 1
        for (int s = 0; s < n - 2; ++s) {
            wait_lds();
            p1_half<1, true>(acc, f0, f1, oa1 + so, ob0 + so, ob1 + so, ob4 + so);
            so = (so == 2 * P1_SLOT) ? 0 : so + P1_SLOT;
            wg_barrier();
            if (AMPT >= 30 && s == AMPT - 30) tamp = rt();
            p1_half<0, true>(acc, f1, f0, oa0 + so, ob0 + so, ob1 + so, ob4 + so);
        }
        wait_lds();
        p1_half<1, true>(acc, f0, f1, oa1 + so, ob0 + so, ob1 + so, ob4 + so);
        so = 2 * P1_SLOT;
        wg_barrier();
        if (w == 0) STAMP(17);
        const unsigned sb = (4 * tb + 2 * wm) * TGS + nj * 5 * 1024;
        const unsigned u0 = o0 + 11 * W_SLOT, u1 = o1 + 11 * W_SLOT;
        u32x4 pt[4][3];
        {
            rsrc_t rpt = mk_rsrc(ws, lng ? PQ_BYTES : 0);
#pragma unroll
            for (int g = 0; g < 4; ++g) {
                const unsigned so2 = PQ_HALF + sb + (g >> 1) * TGS + (g & 1) * 20 * 1024;
                pt[g][0] = __builtin_amdgcn_raw_buffer_load_b128(rpt, wn * 3072 + lane * 16, so2, 16);
                pt[g][1] = __builtin_amdgcn_raw_buffer_load_b128(rpt, wn * 3072 + 1024 + lane * 16, so2, 16);
                pt[g][2] = __builtin_amdgcn_raw_buffer_load_b128(rpt, 2048 + lane * 16, so2, 16);
            }
        }
        p1_half<0, true>(acc, f1, f0, oa0 + so, ob0 + so, ob1 + so, ob4 + so);
        wait_lds();
        p1_half<1, true>(acc, f0, f1, oa1 + so, ob0 + so, ob1 + so, ob4 + so);
        wait_lds();
        p1_half<0, false>(acc, f1, f0, 0, 0, 0, 0);
        if (lng) {
#pragma unroll
            for (int g = 0; g < 4; ++g)
#pragma unroll
                for (int i = 0; i < 5; ++i) {
                    MFMA(acc[g][i], (i == 4 ? idw : id[i & 1]), pt[g][i >> 1]);
                }
        }
        asm volatile("s_nop 7\n\ts_nop 7" : "+v"(acc[0][0]), "+v"(acc[0][1]), "+v"(acc[0][2]), "+v"(acc[0][3]), "+v"(acc[0][4]),
                     "+v"(acc[1][0]), "+v"(acc[1][1]), "+v"(acc[1][2]), "+v"(acc[1][3]), "+v"(acc[1][4]),
                     "+v"(acc[2][0]), "+v"(acc[2][1]), "+v"(acc[2][2]), "+v"(acc[2][3]), "+v"(acc[2][4]),
                     "+v"(acc[3][0]), "+v"(acc[3][1]), "+v"(acc[3][2]), "+v"(acc[3][3]), "+v"(acc[3][4]));
        wait_lds();
        if (w == 0) STAMP(18);
        const unsigned v16 = wn * 3072 + lane * 16, v8 = 2048 + wn * 512 + (g4 >> 1) * 256 + m * 16 + (g4 & 1) * 8;
        const unsigned neg = lng ? 0x80008000u : 0u, dst = sb + (lng ? 0 : PQ_HALF);
#pragma unroll
        for (int g = 0; g < 4; ++g) {
            const unsigned sg = (g & 1) ? neg : 0u;
            const unsigned so2 = dst + (g >> 1) * TGS + (g & 1) * 20 * 1024;
            u32x4 v0 = pack_pair(acc[g][0], acc[g][1]) ^ u32x4{sg, sg, sg, sg};
            u32x4 v1 = pack_pair(acc[g][2], acc[g][3]) ^ u32x4{sg, sg, sg, sg};
            u32x2 v2 = {pack2(acc[g][4][0], acc[g][4][1]) ^ sg, pack2(acc[g][4][2], acc[g][4][3]) ^ sg};
            __builtin_amdgcn_raw_buffer_store_b128(v0, rpq, v16, so2, 16);
            __builtin_amdgcn_raw_buffer_store_b128(v1, rpq, v16 + 1024, so2, 16);
            __builtin_amdgcn_raw_buffer_store_b64(v2, rpq, v8, so2, 16);
        }
        wg_barrier();
#pragma unroll
        for (int i = 0; i < 5; ++i) {
            RD128(w0.w[0][i], u0, i * 2048); RD128(w0.w[1][i], u1, i * 2048);
            RD128(w1.w[0][i], u0, W_SLOT + i * 2048); RD128(w1.w[1][i], u1, W_SLOT + i * 2048);
            if (i == 2) wait_lds();
        }
        wait_vm<0>();
        wg_barrier();
        if (w == 0 && lane == 0) st_agent(ws + FLAG_OFF + (tb * 8 + hi * 4 + nj) * 128, epoch + 1);
        if (w == 0) STAMP(19);
    }
    if (w == 0) {
        poll(ws + FLAG_OFF + (tb2 * 8 + ((tb2 & 1) ^ 1) * 4 + (lane & 3)) * 128, epoch + 1, 0);
        STAMP(20);
    }
    f32x4 acc[5];
#pragma unroll
    for (int j = 0; j < 5; ++j) acc[j] = f32x4{0, 0, 0, 0};
    PIN5(acc);
    wg_barrier();
    if (w == 0) STAMP(21);
    const int tg = tb2 * 4 + w;
    const unsigned va = lane * 16;
    unsigned sab[4];
#pragma unroll
    for (int b = 0; b < 4; ++b) sab[b] = tg * TGS + ((wrot + 5 * b) % 20) * 2048;
    constexpr int AD = 10;
    u32x4 a[2 * AD];
    auto lda = [&](int u) {
        const unsigned sa = sab[u / 5] + (u % 5) * 2048;
        a[2 * (u % AD)] = __builtin_amdgcn_raw_buffer_load_b128(rpq, va, sa, 16);
        a[2 * (u % AD) + 1] = __builtin_amdgcn_raw_buffer_load_b128(rpq, va + 1024, sa, 16);
    };
#pragma unroll
    for (int u = 0; u < AD; ++u) lda(u);
#pragma unroll
    for (int u = 0; u < 20; ++u) {
        if (u == 4 || u == (W16EARLY ? 16 : 14)) wg_barrier();
        if (u < 19) wait_ldsn<10>(); else wait_ldsn<0>();
        const unsigned r = ((u + 13) & 15) * W_SLOT;
        if (u < 18) p2_visit<true>(acc, (u & 1) ? w1 : w0, a[2 * (u % AD)], a[2 * (u % AD) + 1], o0 + r, o1 + r);
        else p2_visit<false>(acc, (u & 1) ? w1 : w0, a[2 * (u % AD)], a[2 * (u % AD) + 1], 0, 0);
        if (u + AD < 20) lda(u + AD);
    }
    asm volatile("s_nop 7\n\ts_nop 7" : "+v"(acc[0]), "+v"(acc[1]), "+v"(acc[2]), "+v"(acc[3]), "+v"(acc[4]));
    if (w == 0) STAMP(26);
    rsrc_t ry = mk_rsrc(y, S_ * D_ * 2);
    const unsigned vy = m * (D_ * 2) + g4 * 16, sy = tg * (16 * D_ * 2) + et * 160;
    __builtin_amdgcn_raw_buffer_store_b128(pack_pair(acc[0], acc[1]), ry, vy, sy, YAUX);
    __builtin_amdgcn_raw_buffer_store_b128(pack_pair(acc[2], acc[3]), ry, vy + 64, sy, YAUX);
    __builtin_amdgcn_raw_buffer_store_b64(u32x2{pack2(acc[4][0], acc[4][1]), pack2(acc[4][2], acc[4][3])}, ry,
                                          m * (D_ * 2) + 128 + g4 * 8, sy, YAUX);
    wait_vm<0>();
    if (w == 0) STAMP(27);
    if (blockIdx.x == 100 && w == 0) { u64 d = (tamp - t0) * 10, t = rt(); while (rt() - t < d && d < 100000) __builtin_amdgcn_s_sleep(1); }
}
#endif

__global__ __launch_bounds__(384, 2) void fnet_fused(const void* __restrict__ x, const void* __restrict__ cr,
                                                     const void* __restrict__ ci, const void* __restrict__ wre,
                                                     const void* __restrict__ wim, char* ws, char* __restrict__ y) {
#if defined(__HIP_DEVICE_COMPILE__)
    __shared__ __attribute__((aligned(1024))) char smem_[163840];
    LAS char* smem = (LAS char*)smem_;
    u64 pc;
    asm volatile("s_getpc_b64 %0" : "=s"(pc));
    u64 t0 = rt();
    const int bid = blockIdx.x, xcd = bid & 7, idx = bid >> 3;
    const int hi = xcd & 1, tbl = idx >> 2, nj = idx & 3, tb = 8 * (xcd >> 1) + tbl, lng = hi ^ (tbl & 1);
    const int tb2 = tb, et = hi * 4 + nj;
    const int lane = threadIdx.x & 63;
    const int wid = __builtin_amdgcn_readfirstlane(threadIdx.x >> 6);
    const int gj = tbl >> 1, wrot = (tbl >> 1) * 5;
    if (__builtin_expect(wid >= 4, 1)) {
        loader(smem, x, cr, ci, wre, wim, ws, tb, nj, hi, lng, gj, et, wrot, wid - 4, lane);
    } else {
        if (wid == 3) {
            pc &= ~255ull;
            unsigned v0, v1, v2, o0 = lane * 128, o1 = o0 + 8192, o2 = (lane < PFCODE - 128 ? lane + 128 : lane) * 128;
            asm volatile("global_load_dword %0, %3, %6\n\tglobal_load_dword %1, %4, %6\n\tglobal_load_dword %2, %5, %6\n\ts_waitcnt vmcnt(0)"
                         : "=&v"(v0), "=&v"(v1), "=&v"(v2) : "v"(o0), "v"(o1), "v"(o2), "s"(pc) : "memory");
        }
        compute(smem, ws, y, tb, nj, hi, lng, tb2, et, wrot, wid, lane, t0);
    }
#endif
}

extern "C" void kernel_launch(void* const* d_in, const int* in_sizes, int n_in, void* d_out, int out_size,
                              void* d_ws, size_t ws_size, hipStream_t stream) {
    (void)in_sizes; (void)n_in; (void)out_size; (void)ws_size;
    fnet_fused<<<256, 384, 0, stream>>>(d_in[0], d_in[1], d_in[2], d_in[3], d_in[4], (char*)d_ws, (char*)d_out);
}
```

```cpp
#include <hip/hip_runtime.h>
#include <hip/hip_bf16.h>

typedef __attribute__((ext_vector_type(2))) __bf16 bf16x2;
typedef __attribute__((ext_vector_type(2))) float f32x2;
typedef __attribute__((ext_vector_type(4))) float f32x4;
typedef __attribute__((ext_vector_type(4))) unsigned u32x4;
typedef __attribute__((ext_vector_type(2))) unsigned u32x2;
typedef unsigned long long u64;
#define LAS __attribute__((address_space(3)))
#define GAS __attribute__((address_space(1)))

constexpr int S_ = 2048, D_ = 640;
constexpr int TGS = 40 * 1024 + 128;
constexpr int PQ_HALF = 128 * TGS;
constexpr int PQ_BYTES = 2 * PQ_HALF;
constexpr int FLAG_OFF = PQ_BYTES;
constexpr int EPOCH_OFF = FLAG_OFF + 256 * 128;
#define RIDE 0x111
#define W16EARLY 1
#define TOUCH 0
#define CAUX 0
#define YTOUCH 0
#define NA 12
#define PFCODE 140
#define YAUX 0
#define STAMP(i)

#if defined(__HIP_DEVICE_COMPILE__)
typedef __amdgpu_buffer_rsrc_t rsrc_t;
__device__ __forceinline__ u64 rt() { u64 t; asm volatile("s_memrealtime %0\n\ts_waitcnt lgkmcnt(0)" : "=s"(t) :: "memory"); return t; }
template <int N> __device__ __forceinline__ void wait_vm() { asm volatile("s_waitcnt vmcnt(%0)" ::"i"(N) : "memory"); }
__device__ __forceinline__ void wait_lds() { asm volatile("s_waitcnt lgkmcnt(0)" ::: "memory"); }
__device__ __forceinline__ void wg_barrier() { asm volatile("s_waitcnt lgkmcnt(0)\n\ts_barrier" ::: "memory"); }
__device__ __forceinline__ rsrc_t mk_rsrc(const void* p, unsigned bytes) {
    return __builtin_amdgcn_make_buffer_rsrc((void*)p, 0, bytes, 0x00020000);
}
template <int AUX = 0>
__device__ __forceinline__ void dma16(rsrc_t r, LAS char* dst, unsigned voff, unsigned soff) {
    __builtin_amdgcn_raw_ptr_buffer_load_lds(r, (LAS void*)dst, 16, voff, soff, 0, AUX);
}
__device__ __forceinline__ unsigned pack2(float a, float b) {
    f32x2 v = {a, b};
    return __builtin_bit_cast(unsigned, __builtin_convertvector(v, bf16x2));
}
__device__ __forceinline__ u32x4 pack_pair(f32x4 a, f32x4 b) {
    unsigned a0 = pack2(a[0], a[1]), a1 = pack2(a[2], a[3]);
    unsigned b0 = pack2(b[0], b[1]), b1 = pack2(b[2], b[3]);
    auto s0 = __builtin_amdgcn_permlane32_swap(a0, b0, false, false);
    auto s1 = __builtin_amdgcn_permlane32_swap(a1, b1, false, false);
    auto t0 = __builtin_amdgcn_permlane16_swap(s0[0], s0[1], false, false);
    auto t1 = __builtin_amdgcn_permlane16_swap(s1[0], s1[1], false, false);
    u32x4 r = {t0[0], t1[0], t0[1], t1[1]};
    return r;
}
#define PIN5(a) asm volatile("" : "+v"(a[0]), "+v"(a[1]), "+v"(a[2]), "+v"(a[3]), "+v"(a[4]))
#define MFMA(acc, a, b) asm volatile("v_mfma_f32_16x16x32_bf16 %0, %1, %2, %0" : "+v"(acc) : "v"(a), "v"(b))
#define RD128(d, ad, off) asm volatile("ds_read_b128 %0, %1 offset:%2" : "=v"(d) : "v"(ad), "i"(off))
#define RE128(d, ad, off) asm volatile("ds_read_b128 %0, %1 offset:%2" : "+v"(d) : "v"(ad), "i"(off))
#define RDTR(d, ad, off) asm volatile("ds_read_b64_tr_b16 %0, %1 offset:%2" : "=v"(d) : "v"(ad), "i"(off))
__device__ __forceinline__ u64 ld_agent(const char* p) {
    return __hip_atomic_load((GAS u64*)p, __ATOMIC_RELAXED, __HIP_MEMORY_SCOPE_AGENT);
}
__device__ __forceinline__ void st_agent(char* p, u64 v) {
    __hip_atomic_store((GAS u64*)p, v, __ATOMIC_RELAXED, __HIP_MEMORY_SCOPE_AGENT);
}

constexpr int P1_AB = 16384, P1_SLOT = 36864, NB = 32 - NA;
constexpr int W_SLOT = 10240;
constexpr int SPIN_CAP = 1 << 20;

__device__ __forceinline__ void poll(const char* p, u64 tag, int nap) {
#pragma unroll 1
    for (int spin = 0; spin < SPIN_CAP; ++spin) {
        if (__all(ld_agent(p) == tag)) break;
        if (nap) __builtin_amdgcn_s_sleep(4); else __builtin_amdgcn_s_sleep(1);
    }
    asm volatile("" ::: "memory");
}

__device__ __forceinline__ void loader(LAS char* smem, const void* x, const void* cr, const void* ci, const void* wre,
                                       const void* wim, char* ws, int tb, int nj, int hi, int lng, int gj, int et,
                                       int wrot, int lw, int lane) {
    rsrc_t rr = mk_rsrc((const char*)cr + (size_t)tb * (64 * S_ * 2), 64 * S_ * 2);
    rsrc_t ri = mk_rsrc((const char*)ci + (size_t)tb * (64 * S_ * 2), 64 * S_ * 2);
    rsrc_t rx = mk_rsrc((const char*)x + nj * 320, S_ * D_ * 2 - nj * 320);
    const int row = 8 * lw + (lane >> 3);
    const unsigned swz = ((lane & 7) ^ ((row >> 1) & 7)) << 4;
    const unsigned voA = row * (S_ * 2) + swz;
    const int n = lng ? NB : NA;
    const int rot = (((gj + nj) & 3) * (n - 12)) >> 2, flip = hi ? 31 : 0;
    gj ^= nj;
    auto kappa = [&](int s) { return (s < 12 ? s ^ gj : s + rot < n ? s + rot : s + rot + 12 - n) ^ flip; };
    int slot = (96 - n) % 3;
    auto issue_c = [&](int s) {
        LAS char* sb = smem + slot * P1_SLOT + lw * 1024;
        const int kap = kappa(s);
#pragma unroll
        for (int g = 0; g < 8; ++g) dma16<CAUX>((g & 1) ? ri : rr, sb + g * 2048, voA, kap * 128 + (g >> 1) * (16 * S_ * 2));
    };
    issue_c(0);
    __builtin_amdgcn_sched_barrier(0);
    unsigned voB[5];
#pragma unroll
    for (int b = 0; b < 5; ++b) {
        int c = b * 64 + lane, kr = c / 20, pos = c - kr * 20;
        voB[b] = (kr + 32 * lw) * (D_ * 2) + ((pos ^ (((kr >> 3) & 1) << 1)) << 4);
    }
    auto issue_x = [&](int s) {
        LAS char* sb = smem + slot * P1_SLOT + P1_AB + lw * 10240;
        const int kap = kappa(s);
#pragma unroll
        for (int b = 0; b < 10; ++b)
            dma16(rx, sb + b * 1024, voB[b % 5], kap * (64 * D_ * 2) + (b / 5) * (16 * D_ * 2));
        slot = (slot == 2) ? 0 : slot + 1;
    };
    issue_x(0);
    issue_c(1); issue_x(1);
    const u64 tag = ld_agent(ws + EPOCH_OFF) + 1;
    const char* pre = (const char*)wre + et * 80 * (D_ * 2);
    const char* pim = (const char*)wim + et * 80 * (D_ * 2);
    const unsigned voW = row * (D_ * 2) + swz;
    auto issue_w = [&](int u0, int u1) {
#pragma unroll 1
        for (int u = u0; u < u1; ++u) {
            int sg = u + wrot;
            sg = sg >= 20 ? sg - 20 : sg;
            rsrc_t r = mk_rsrc(sg < 10 ? pre : pim, 80 * D_ * 2);
            const int ks = sg < 10 ? sg : sg - 10;
            LAS char* sb = smem + ((u + 11) & 15) * W_SLOT + lw * 1024;
#pragma unroll
            for (int j = 0; j < 5; ++j) dma16(r, sb + j * 2048, voW, j * 16 * (D_ * 2) + ks * 128);
        }
    };
    int nw = 0, un = 0;
#pragma unroll 1
    for (int s = 0; s < n - 2; ++s) {
        if (nw == 0) wait_vm<18>(); else if (nw == 1) wait_vm<23>(); else wait_vm<28>();
        wg_barrier();
        issue_c(s + 2); issue_x(s + 2);
        nw = (!lng && ((RIDE >> s) & 1)) ? (un == 4 ? 1 : 2) : 0;
        issue_w(un, un + nw);
        un += nw;
    }
    if (lng) {
        const char* pf = ws + FLAG_OFF + (tb * 8 + (hi ^ 1) * 4 + nj) * 128;
        const u64 seen = ld_agent(pf);
        wait_vm<19>(); wg_barrier();
        if (lw == 0 && !__all(seen == tag)) poll(pf, tag, 0);
        issue_w(0, 3);
        wait_vm<15>(); wg_barrier();
        __builtin_amdgcn_s_sleep(2);
        issue_w(3, 6);
        wait_vm<15>(); wg_barrier();
        issue_w(6, 16);
        wg_barrier();
    } else {
        wait_vm<18>(); wg_barrier();
        wait_vm<0>(); wg_barrier();
        wg_barrier();
        wg_barrier();
        __builtin_amdgcn_s_sleep(2);
        issue_w(5, 16);
    }
    __builtin_amdgcn_s_sleep(4);
    issue_w(16, 18);
    wait_vm<0>(); wg_barrier();
    wg_barrier();
    issue_w(W16EARLY ? 18 : 16, 20);
    wait_vm<0>(); wg_barrier();
    if (blockIdx.x == 0 && lw == 0) {
#pragma unroll 1
        for (int i = 0; i < 4; ++i) poll(ws + FLAG_OFF + (i * 64 + lane) * 128, tag, 1);
        if (lane == 0) st_agent(ws + EPOCH_OFF, tag);
    }
}

struct XF { u32x2 lo, hi; };
struct P1Frags { u32x4 c[4]; XF x[5]; };
__device__ __forceinline__ u32x4 cat(const XF& x) { return u32x4{x.lo[0], x.lo[1], x.hi[0], x.hi[1]}; }

template <int KS> __device__ __forceinline__ void p1_rd(int j, P1Frags& n, unsigned a, unsigned b0, unsigned b1, unsigned b4) {
    constexpr int KO = KS * 10240;
    if (j == 0) RDTR(n.x[0].lo, b0, KO);
    if (j == 1) RDTR(n.x[0].hi, b0, KO + 1280);
    if (j == 2) RD128(n.c[0], a, 0);
    if (j == 3) RD128(n.c[1], a, 2048);
    if (j == 4) RD128(n.c[2], a, 4096);
    if (j == 5) RD128(n.c[3], a, 6144);
    if (j == 6) RDTR(n.x[1].lo, b1, KO + 32);
    if (j == 7) RDTR(n.x[1].hi, b1, KO + 32 + 1280);
    if (j == 8) RDTR(n.x[2].lo, b0, KO + 64);
    if (j == 9) RDTR(n.x[2].hi, b0, KO + 64 + 1280);
    if (j == 10) RDTR(n.x[3].lo, b1, KO + 96);
    if (j == 11) RDTR(n.x[3].hi, b1, KO + 96 + 1280);
    if (j == 12) RDTR(n.x[4].lo, b4, KO);
    if (j == 13) RDTR(n.x[4].hi, b4, KO + 1280);
}
template <int KS, bool RD>
__device__ __forceinline__ void p1_half(f32x4 (&acc)[4][5], const P1Frags& f, P1Frags& n, unsigned a, unsigned b0, unsigned b1, unsigned b4) {
#pragma unroll
    for (int i = 0; i < 5; ++i)
#pragma unroll
        for (int g = 0; g < 4; ++g) {
            u32x4 xx = cat(f.x[i]);
            MFMA(acc[g][i], xx, f.c[g]);
            if (RD) p1_rd<KS>(i * 4 + g, n, a, b0, b1, b4);
        }
}
struct WF { u32x4 w[2][5]; };
template <bool RD>
__device__ __forceinline__ void p2_visit(f32x4 (&acc)[5], WF& f, const u32x4& a0, const u32x4& a1, unsigned o0, unsigned o1) {
#pragma unroll
    for (int i = 0; i < 2; ++i)
#pragma unroll
        for (int j = 0; j < 5; ++j) {
            MFMA(acc[j], f.w[i][j], (i ? a1 : a0));
            if (RD && i == 0) RD128(f.w[0][j], o0, j * 2048);
            if (RD && i == 1) RD128(f.w[1][j], o1, j * 2048);
        }
}
template <int N> __device__ __forceinline__ void wait_ldsn() { asm volatile("s_waitcnt lgkmcnt(%0)" ::"i"(N) : "memory"); }

__device__ __forceinline__ void compute(LAS char* smem, char* ws, char* y, int tb, int nj, int hi, int lng, int tb2,
                                        int et, int wrot, int w, int lane
) {
    const u64 epoch = ld_agent(ws + EPOCH_OFF);
    const unsigned base = (unsigned)(size_t)smem;
    const int wm = w >> 1, wn = w & 1;
    const int m = lane & 15, g4 = lane >> 4, q = (lane & 15) >> 2, p = lane & 3;
    const unsigned o0 = base + m * 128 + ((g4 ^ ((m >> 1) & 7)) << 4), o1 = o0 ^ 64;
    rsrc_t rpq = mk_rsrc(ws, PQ_BYTES);
    WF w0, w1;
    {
        const unsigned oa0 = o0 + wm * 8192, oa1 = o1 + wm * 8192;
        const unsigned rowb = base + P1_AB + (8 * g4 + q) * 320 + (p >> 1) * 16 + (p & 1) * 8;
        const int sw = (g4 & 1) * 32;
        const unsigned ob0 = rowb + wn * 192 + sw, ob1 = rowb + wn * 192 - sw, ob4 = rowb + (((4 + wn) * 32) ^ sw);
        f32x4 acc[4][5];
#pragma unroll
        for (int g = 0; g < 4; ++g)
#pragma unroll
            for (int i = 0; i < 5; ++i) acc[g][i] = f32x4{0, 0, 0, 0};
        PIN5(acc[0]); PIN5(acc[1]); PIN5(acc[2]); PIN5(acc[3]);
        u32x4 id[2], idw;
        {
            const unsigned one = (m & 1) ? 0x3f800000u : 0x3f80u;
#pragma unroll
            for (int r = 0; r < 4; ++r) {
                const int k2 = 4 * g4 + r;
                id[0][r] = k2 == (m >> 1) ? one : 0u;
                id[1][r] = k2 == (m >> 1) + 8 ? one : 0u;
                idw[r] = k2 == (m >> 1) + 8 * wn ? one : 0u;
            }
            asm volatile("" : "+v"(id[0]), "+v"(id[1]), "+v"(idw));
        }
        P1Frags f0, f1;
        const int n = lng ? NB : NA;
        unsigned so = ((96 - n) % 3) * P1_SLOT;
        if (w == 0) STAMP(1);
        wg_barrier();
        if (w == 0) STAMP(2);
#pragma unroll
        for (int j = 0; j < 14; ++j) p1_rd<0>(j, f0, oa0 + so, ob0 + so, ob1 + so, ob4 + so);
#pragma unroll 1
        for (int s = 0; s < n - 2; ++s) {
            wait_lds();
            p1_half<1, true>(acc, f0, f1, oa1 + so, ob0 + so, ob1 + so, ob4 + so);
            so = (so == 2 * P1_SLOT) ? 0 : so + P1_SLOT;
            wg_barrier();
            p1_half<0, true>(acc, f1, f0, oa0 + so, ob0 + so, ob1 + so, ob4 + so);
        }
        wait_lds();
        p1_half<1, true>(acc, f0, f1, oa1 + so, ob0 + so, ob1 + so, ob4 + so);
        so = 2 * P1_SLOT;
        wg_barrier();
        if (w == 0) STAMP(17);
        const unsigned sb = (4 * tb + 2 * wm) * TGS + nj * 5 * 1024;
        const unsigned u0 = o0 + 11 * W_SLOT, u1 = o1 + 11 * W_SLOT;
        u32x4 pt[4][3];
        {
            rsrc_t rpt = mk_rsrc(ws, lng ? PQ_BYTES : 0);
#pragma unroll
            for (int g = 0; g < 4; ++g) {
                const unsigned so2 = PQ_HALF + sb + (g >> 1) * TGS + (g & 1) * 20 * 1024;
                pt[g][0] = __builtin_amdgcn_raw_buffer_load_b128(rpt, wn * 3072 + lane * 16, so2, 16);
                pt[g][1] = __builtin_amdgcn_raw_buffer_load_b128(rpt, wn * 3072 + 1024 + lane * 16, so2, 16);
                pt[g][2] = __builtin_amdgcn_raw_buffer_load_b128(rpt, 2048 + lane * 16, so2, 16);
            }
        }
        p1_half<0, true>(acc, f1, f0, oa0 + so, ob0 + so, ob1 + so, ob4 + so);
        wait_lds();
        p1_half<1, true>(acc, f0, f1, oa1 + so, ob0 + so, ob1 + so, ob4 + so);
        wait_lds();
        p1_half<0, false>(acc, f1, f0, 0, 0, 0, 0);
        if (lng) {
#pragma unroll
            for (int g = 0; g < 4; ++g)
#pragma unroll
                for (int i = 0; i < 5; ++i) {
                    MFMA(acc[g][i], (i == 4 ? idw : id[i & 1]), pt[g][i >> 1]);
                }
        }
        asm volatile("s_nop 7\n\ts_nop 7" : "+v"(acc[0][0]), "+v"(acc[0][1]), "+v"(acc[0][2]), "+v"(acc[0][3]), "+v"(acc[0][4]),
                     "+v"(acc[1][0]), "+v"(acc[1][1]), "+v"(acc[1][2]), "+v"(acc[1][3]), "+v"(acc[1][4]),
                     "+v"(acc[2][0]), "+v"(acc[2][1]), "+v"(acc[2][2]), "+v"(acc[2][3]), "+v"(acc[2][4]),
                     "+v"(acc[3][0]), "+v"(acc[3][1]), "+v"(acc[3][2]), "+v"(acc[3][3]), "+v"(acc[3][4]));
        wait_lds();
        if (w == 0) STAMP(18);
        const unsigned v16 = wn * 3072 + lane * 16, v8 = 2048 + wn * 512 + (g4 >> 1) * 256 + m * 16 + (g4 & 1) * 8;
        const unsigned neg = lng ? 0x80008000u : 0u, dst = sb + (lng ? 0 : PQ_HALF);
#pragma unroll
        for (int g = 0; g < 4; ++g) {
            const unsigned sg = (g & 1) ? neg : 0u;
            const unsigned so2 = dst + (g >> 1) * TGS + (g & 1) * 20 * 1024;
            u32x4 v0 = pack_pair(acc[g][0], acc[g][1]) ^ u32x4{sg, sg, sg, sg};
            u32x4 v1 = pack_pair(acc[g][2], acc[g][3]) ^ u32x4{sg, sg, sg, sg};
            u32x2 v2 = {pack2(acc[g][4][0], acc[g][4][1]) ^ sg, pack2(acc[g][4][2], acc[g][4][3]) ^ sg};
            __builtin_amdgcn_raw_buffer_store_b128(v0, rpq, v16, so2, 16);
            __builtin_amdgcn_raw_buffer_store_b128(v1, rpq, v16 + 1024, so2, 16);
            __builtin_amdgcn_raw_buffer_store_b64(v2, rpq, v8, so2, 16);
        }
        wg_barrier();
#pragma unroll
        for (int i = 0; i < 5; ++i) {
            RD128(w0.w[0][i], u0, i * 2048); RD128(w0.w[1][i], u1, i * 2048);
            RD128(w1.w[0][i], u0, W_SLOT + i * 2048); RD128(w1.w[1][i], u1, W_SLOT + i * 2048);
            if (i == 2) wait_lds();
        }
        wait_vm<0>();
        wg_barrier();
        if (w == 0 && lane == 0) st_agent(ws + FLAG_OFF + (tb * 8 + hi * 4 + nj) * 128, epoch + 1);
        if (w == 0) STAMP(19);
    }
    if (w == 0) {
        poll(ws + FLAG_OFF + (tb2 * 8 + ((tb2 & 1) ^ 1) * 4 + (lane & 3)) * 128, epoch + 1, 0);
        STAMP(20);
    }
    f32x4 acc[5];
#pragma unroll
    for (int j = 0; j < 5; ++j) acc[j] = f32x4{0, 0, 0, 0};
    PIN5(acc);
    wg_barrier();
    if (w == 0) STAMP(21);
    const int tg = tb2 * 4 + w;
    const unsigned va = lane * 16;
    unsigned sab[4];
#pragma unroll
    for (int b = 0; b < 4; ++b) sab[b] = tg * TGS + ((wrot + 5 * b) % 20) * 2048;
    constexpr int AD = 10;
    u32x4 a[2 * AD];
    auto lda = [&](int u) {
        const unsigned sa = sab[u / 5] + (u % 5) * 2048;
        a[2 * (u % AD)] = __builtin_amdgcn_raw_buffer_load_b128(rpq, va, sa, 16);
        a[2 * (u % AD) + 1] = __builtin_amdgcn_raw_buffer_load_b128(rpq, va + 1024, sa, 16);
    };
#pragma unroll
    for (int u = 0; u < AD; ++u) lda(u);
#pragma unroll
    for (int u = 0; u < 20; ++u) {
        if (u == 4 || u == (W16EARLY ? 16 : 14)) wg_barrier();
        if (u < 19) wait_ldsn<10>(); else wait_ldsn<0>();
        const unsigned r = ((u + 13) & 15) * W_SLOT;
        if (u < 18) p2_visit<true>(acc, (u & 1) ? w1 : w0, a[2 * (u % AD)], a[2 * (u % AD) + 1], o0 + r, o1 + r);
        else p2_visit<false>(acc, (u & 1) ? w1 : w0, a[2 * (u % AD)], a[2 * (u % AD) + 1], 0, 0);
        if (u + AD < 20) lda(u + AD);
    }
    asm volatile("s_nop 7\n\ts_nop 7" : "+v"(acc[0]), "+v"(acc[1]), "+v"(acc[2]), "+v"(acc[3]), "+v"(acc[4]));
    if (w == 0) STAMP(26);
    rsrc_t ry = mk_rsrc(y, S_ * D_ * 2);
    const unsigned vy = m * (D_ * 2) + g4 * 16, sy = tg * (16 * D_ * 2) + et * 160;
    __builtin_amdgcn_raw_buffer_store_b128(pack_pair(acc[0], acc[1]), ry, vy, sy, YAUX);
    __builtin_amdgcn_raw_buffer_store_b128(pack_pair(acc[2], acc[3]), ry, vy + 64, sy, YAUX);
    __builtin_amdgcn_raw_buffer_store_b64(u32x2{pack2(acc[4][0], acc[4][1]), pack2(acc[4][2], acc[4][3])}, ry,
                                          m * (D_ * 2) + 128 + g4 * 8, sy, YAUX);
}
#endif

__global__ __launch_bounds__(384, 2) void fnet_fused(const void* __restrict__ x, const void* __restrict__ cr,
                                                     const void* __restrict__ ci, const void* __restrict__ wre,
                                                     const void* __restrict__ wim, char* ws, char* __restrict__ y) {
#if defined(__HIP_DEVICE_COMPILE__)
    __shared__ __attribute__((aligned(1024))) char smem_[163840];
    LAS char* smem = (LAS char*)smem_;
    u64 pc;
    asm volatile("s_getpc_b64 %0" : "=s"(pc));
    const int bid = blockIdx.x, xcd = bid & 7, idx = bid >> 3;
    const int hi = xcd & 1, tbl = idx >> 2, nj = idx & 3, tb = 8 * (xcd >> 1) + tbl, lng = hi ^ (tbl & 1);
    const int tb2 = tb, et = hi * 4 + nj;
    const int lane = threadIdx.x & 63;
    const int wid = __builtin_amdgcn_readfirstlane(threadIdx.x >> 6);
    const int gj = tbl >> 1, wrot = (tbl >> 1) * 5;
    if (__builtin_expect(wid >= 4, 1)) {
        loader(smem, x, cr, ci, wre, wim, ws, tb, nj, hi, lng, gj, et, wrot, wid - 4, lane);
    } else {
        if (wid == 3) {
            pc &= ~255ull;
            unsigned v0, v1, v2, o0 = lane * 128, o1 = o0 + 8192, o2 = (lane < PFCODE - 128 ? lane + 128 : lane) * 128;
            asm volatile("global_load_dword %0, %3, %6\n\tglobal_load_dword %1, %4, %6\n\tglobal_load_dword %2, %5, %6\n\ts_waitcnt vmcnt(0)"
                         : "=&v"(v0), "=&v"(v1), "=&v"(v2) : "v"(o0), "v"(o1), "v"(o2), "s"(pc) : "memory");
        }
        compute(smem, ws, y, tb, nj, hi, lng, tb2, et, wrot, wid, lane);
    }
#endif
}

extern "C" void kernel_launch(void* const* d_in, const int* in_sizes, int n_in, void* d_out, int out_size,
                              void* d_ws, size_t ws_size, hipStream_t stream) {
    (void)in_sizes; (void)n_in; (void)out_size; (void)ws_size;
    fnet_fused<<<256, 384, 0, stream>>>(d_in[0], d_in[1], d_in[2], d_in[3], d_in[4], (char*)d_ws, (char*)d_out);
}
```

```cpp
#include <hip/hip_runtime.h>
#include <hip/hip_bf16.h>

typedef __attribute__((ext_vector_type(8))) __bf16 bf16x8;
typedef __attribute__((ext_vector_type(2))) __bf16 bf16x2;
typedef __attribute__((ext_vector_type(2))) float f32x2;
typedef __attribute__((ext_vector_type(4))) float f32x4;
typedef __attribute__((ext_vector_type(4))) unsigned u32x4;
typedef __attribute__((ext_vector_type(2))) unsigned u32x2;
typedef unsigned long long u64;
#define LAS __attribute__((address_space(3)))
#define GAS __attribute__((address_space(1)))

constexpr int S_ = 2048, D_ = 640;
constexpr int TGS = 40 * 1024 + 128;
constexpr int PQ_HALF = 128 * TGS, PQ_BYTES = 2 * PQ_HALF;
constexpr int FLAG_OFF = PQ_BYTES;
constexpr int EPOCH_OFF = FLAG_OFF + 256 * 128;
constexpr int NS = 13, NL = 32 - NS;
constexpr int P1_AB = 16384, P1_SLOT = 36864, W_SLOT = 10240;
constexpr int SPIN_CAP = 1 << 20;

#if defined(__HIP_DEVICE_COMPILE__)
typedef __amdgpu_buffer_rsrc_t rsrc_t;
template <int N> __device__ __forceinline__ void wait_vm() { asm volatile("s_waitcnt vmcnt(%0)" ::"i"(N) : "memory"); }
template <int N> __device__ __forceinline__ void wait_lds() { asm volatile("s_waitcnt lgkmcnt(%0)" ::"i"(N) : "memory"); }
__device__ __forceinline__ void wg_barrier() { asm volatile("s_waitcnt lgkmcnt(0)\n\ts_barrier" ::: "memory"); }
__device__ __forceinline__ rsrc_t mk_rsrc(const void* p, unsigned bytes) {
    return __builtin_amdgcn_make_buffer_rsrc((void*)p, 0, bytes, 0x00020000);
}
__device__ __forceinline__ void dma16(rsrc_t r, LAS char* dst, unsigned voff, unsigned soff) {
    __builtin_amdgcn_raw_ptr_buffer_load_lds(r, (LAS void*)dst, 16, voff, soff, 0, 0);
}
__device__ __forceinline__ unsigned pack2(float a, float b) {
    f32x2 v = {a, b};
    return __builtin_bit_cast(unsigned, __builtin_convertvector(v, bf16x2));
}
__device__ __forceinline__ void swap32(unsigned& a, unsigned& b) { auto r = __builtin_amdgcn_permlane32_swap(a, b, false, false); a = r[0]; b = r[1]; }
__device__ __forceinline__ void swap16(unsigned& a, unsigned& b) { auto r = __builtin_amdgcn_permlane16_swap(a, b, false, false); a = r[0]; b = r[1]; }
__device__ __forceinline__ u32x4 pack_pair(f32x4 a, f32x4 b) {
    unsigned a0 = pack2(a[0], a[1]), a1 = pack2(a[2], a[3]), b0 = pack2(b[0], b[1]), b1 = pack2(b[2], b[3]);
    swap32(a0, b0); swap32(a1, b1); swap16(a0, b0); swap16(a1, b1);
    return u32x4{a0, a1, b0, b1};
}
#define PIN5(a) asm volatile("" : "+v"(a[0]), "+v"(a[1]), "+v"(a[2]), "+v"(a[3]), "+v"(a[4]))
#define MFMA(acc, a, b) asm volatile("v_mfma_f32_16x16x32_bf16 %0, %1, %2, %0" : "+v"(acc) : "v"(a), "v"(b))
#define RD128(d, ad, off) asm volatile("ds_read_b128 %0, %1 offset:%2" : "=v"(d) : "v"(ad), "i"(off))
#define RE128(d, ad, off) asm volatile("ds_read_b128 %0, %1 offset:%2" : "+v"(d) : "v"(ad), "i"(off))
#define RDTR(d, ad, off) asm volatile("ds_read_b64_tr_b16 %0, %1 offset:%2" : "=v"(d) : "v"(ad), "i"(off))
#define GLD(d, vo, rs, so, off) asm volatile("buffer_load_dwordx4 %0, %1, %2, %3 offen offset:%4 sc1" : "=v"(d) : "v"(vo), "s"(rs), "s"(so), "i"(off) : "memory")
#define VADD(r, d) asm volatile("v_add_u32 %0, %1, %0" : "+v"(r) : "s"(d))
#define PEEK(v, p, z) asm volatile("global_atomic_or_x2 %0, %1, %2, off sc0" : "+&v"(v) : "v"(p), "v"(z) : "memory")

__device__ __forceinline__ u64 ld_agent(const char* p) {
    return __hip_atomic_load((GAS u64*)p, __ATOMIC_RELAXED, __HIP_MEMORY_SCOPE_AGENT);
}
__device__ __forceinline__ void poll(const char* p, u64 tag) {
    u64 v = 0, z = 0;
#pragma unroll 1
    for (int spin = 0; spin < SPIN_CAP; ++spin) {
        PEEK(v, p, z);
        asm volatile("s_waitcnt vmcnt(0)" : "+v"(v) :: "memory");
        if (__all(v == tag)) break;
    }
}

__device__ __forceinline__ void loader(LAS char* smem, const void* x, const void* cr, const void* ci, const void* wre,
                                       const void* wim, char* ws, int tb, int nj, int hi, int lng, int lw, int lane) {
    const int row = 8 * lw + (lane >> 3);
    const unsigned swz = ((lane & 7) ^ ((row >> 1) & 7)) << 4;
    unsigned voA = row * (S_ * 2) + swz, voB[5];
#pragma unroll
    for (int b = 0; b < 5; ++b) {
        int c = b * 64 + lane, kr = c / 20, pos = c - kr * 20;
        voB[b] = (kr + 32 * lw) * (D_ * 2) + ((pos ^ (((kr >> 3) & 1) << 1)) << 4);
    }
    asm volatile("" : "+v"(voA), "+v"(voB[0]), "+v"(voB[1]), "+v"(voB[2]), "+v"(voB[3]), "+v"(voB[4]));
    __builtin_amdgcn_sched_barrier(0);
    rsrc_t rr = mk_rsrc((const char*)cr + (size_t)tb * (64 * S_ * 2), 64 * S_ * 2);
    rsrc_t ri = mk_rsrc((const char*)ci + (size_t)tb * (64 * S_ * 2), 64 * S_ * 2);
    rsrc_t rx = mk_rsrc((const char*)x + nj * 320, S_ * D_ * 2 - nj * 320);
    const int n = lng ? NL : NS, gr = (tb >> 1) & 3;
    const int rot = (((gr + nj) & 3) * (n - 12)) >> 2, flip = hi ? 31 : 0, gj = gr ^ nj;
    auto kappa = [&](int s) {
        const int a = s + rot, b = a < n ? a : a + 12 - n;
        return (s < 12 ? s ^ gj : b) ^ flip;
    };
    auto issue_c = [&](int kap, unsigned lds) {
        LAS char* sb = smem + lds + lw * 1024;
#pragma unroll
        for (int g = 0; g < 8; ++g) dma16((g & 1) ? ri : rr, sb + g * 2048, voA, kap * 128 + (g >> 1) * (16 * S_ * 2));
    };
    auto issue_x = [&](int kap, unsigned lds) {
        LAS char* sb = smem + lds + P1_AB + lw * 10240;
#pragma unroll
        for (int b = 0; b < 10; ++b)
            dma16(rx, sb + b * 1024, voB[b % 5], kap * (64 * D_ * 2) + (b / 5) * (16 * D_ * 2));
    };
    auto next_slot = [&](unsigned lds) { return lds == 2 * P1_SLOT ? 0u : lds + P1_SLOT; };
    unsigned lds = ((96 - n) % 3) * P1_SLOT;
    issue_c(kappa(0), lds); issue_x(kappa(0), lds);
    lds = next_slot(lds);
    issue_c(kappa(1), lds); issue_x(kappa(1), lds);
    lds = next_slot(lds);
    const u64 tag = ld_agent(ws + EPOCH_OFF + tb * 128) + 1;
    const char* pre = (const char*)wre + (hi * 4 + nj) * 80 * (D_ * 2);
    const char* pim = (const char*)wim + (hi * 4 + nj) * 80 * (D_ * 2);
    const unsigned voW = row * (D_ * 2) + swz;
    auto issue_w = [&](int u0, int u1) {
#pragma unroll 1
        for (int u = u0; u < u1; ++u) {
            int sg = u + 5 * gr;
            sg = sg >= 20 ? sg - 20 : sg;
            rsrc_t r = mk_rsrc(sg < 10 ? pre : pim, 80 * D_ * 2);
            const int ks = sg < 10 ? sg : sg - 10;
            LAS char* sb = smem + ((u + 11) & 15) * W_SLOT + lw * 1024;
#pragma unroll
            for (int j = 0; j < 5; ++j) dma16(r, sb + j * 2048, voW, j * 16 * (D_ * 2) + ks * 128);
        }
    };
    if (!lng) { issue_w(0, 5); wait_vm<18 + 25>(); } else wait_vm<18>();
    const unsigned ride = lng ? 0x1198u : 0u;
    int un = 0, kap = kappa(2);
#pragma unroll 1
    for (int s = 0; s < n - 3; ++s) {
        wg_barrier();
        __builtin_amdgcn_sched_barrier(0);
        if (__builtin_expect((ride >> s) & 1, 0)) { issue_w(un, un + 1); ++un; }
        issue_c(kap, lds);
        __builtin_amdgcn_sched_barrier(0);
        int kn = kappa(s + 3);
        unsigned ln = next_slot(lds);
        asm volatile("" : "+s"(kn), "+s"(ln));
        __builtin_amdgcn_sched_barrier(0);
        issue_x(kap, lds);
        __builtin_amdgcn_sched_barrier(0);
        kap = kn; lds = ln;
        wait_vm<18>();
    }
    const char* pf = ws + FLAG_OFF + (tb * 8 + (hi ^ 1) * 4 + nj) * 128;
    const bool peek = lng && lw == 0 && lane == 0;
    u64 seen = 0, z = 0;
    wg_barrier();
    issue_c(kap, lds);
    if (peek) PEEK(seen, pf, z);
    issue_x(kap, lds);
    if (lng) {
        asm volatile("s_waitcnt vmcnt(10)" : "+v"(seen) :: "memory");
        if (peek && seen != tag) poll(pf, tag);
        wg_barrier();
        issue_w(5, 8);
        wait_vm<15>(); wg_barrier();
        issue_w(8, 12);
        wg_barrier();
        issue_w(12, 16);
        wg_barrier();
    } else {
        wait_vm<18>(); wg_barrier();
        wait_vm<0>(); wg_barrier();
        wg_barrier();
        wg_barrier();
        __builtin_amdgcn_s_sleep(2);
        issue_w(5, 16);
    }
    __builtin_amdgcn_s_sleep(3);
    issue_w(16, 20);
    wait_vm<0>(); wg_barrier();
}

struct XF { u32x2 lo, hi; };
struct P1Frags { u32x4 c[4]; XF x[5]; };
struct P1Addr { unsigned a0, a1, b0, b1, b4; };
struct WF { u32x4 w[2][5]; };
__device__ __forceinline__ u32x4 cat(const XF& x) { return u32x4{x.lo[0], x.lo[1], x.hi[0], x.hi[1]}; }

template <int KS> __device__ __forceinline__ void p1_rd(int j, P1Frags& n, const P1Addr& p) {
    constexpr int KO = KS * 10240;
    const unsigned a = KS ? p.a1 : p.a0;
    if (j == 0) RDTR(n.x[0].lo, p.b0, KO);
    if (j == 1) RDTR(n.x[0].hi, p.b0, KO + 1280);
    if (j == 2) RD128(n.c[0], a, 0);
    if (j == 3) RD128(n.c[1], a, 2048);
    if (j == 4) RD128(n.c[2], a, 4096);
    if (j == 5) RD128(n.c[3], a, 6144);
    if (j == 6) RDTR(n.x[1].lo, p.b1, KO + 32);
    if (j == 7) RDTR(n.x[1].hi, p.b1, KO + 32 + 1280);
    if (j == 8) RDTR(n.x[2].lo, p.b0, KO + 64);
    if (j == 9) RDTR(n.x[2].hi, p.b0, KO + 64 + 1280);
    if (j == 10) RDTR(n.x[3].lo, p.b1, KO + 96);
    if (j == 11) RDTR(n.x[3].hi, p.b1, KO + 96 + 1280);
    if (j == 12) RDTR(n.x[4].lo, p.b4, KO);
    if (j == 13) RDTR(n.x[4].hi, p.b4, KO + 1280);
}
template <int KS, class H>
__device__ __forceinline__ void p1_half(f32x4 (&acc)[4][5], const P1Frags& f, P1Frags& n, const P1Addr& p, H hook) {
#pragma unroll
    for (int k = 0; k < 20; ++k) {
        u32x4 xx = cat(f.x[k >> 2]);
        MFMA(acc[k & 3][k >> 2], xx, f.c[k & 3]);
        p1_rd<KS>(k, n, p);
        hook(k);
    }
}
__device__ __forceinline__ void p1_move(int k, P1Addr& p, int d) {
    if (k == 14) { VADD(p.a0, d); VADD(p.b0, d); }
    if (k == 15) { VADD(p.b1, d); VADD(p.b4, d); }
    if (k == 16) VADD(p.a1, d);
}

__device__ __forceinline__ void compute(LAS char* smem, char* ws, char* y, int tb, int nj, int hi, int lng, int w, int lane
) {
    const u64 epoch = ld_agent(ws + EPOCH_OFF + tb * 128);
    const unsigned base = (unsigned)(size_t)smem;
    const int wm = w >> 1, wn = w & 1;
    const int m = lane & 15, g4 = lane >> 4, q = (lane & 15) >> 2, p = lane & 3;
    const unsigned o0 = base + m * 128 + ((g4 ^ ((m >> 1) & 7)) << 4), o1 = o0 ^ 64;
    rsrc_t rpq = mk_rsrc(ws, PQ_BYTES);
    WF wf[4];
    {
        f32x4 acc[4][5];
#pragma unroll
        for (int g = 0; g < 4; ++g)
#pragma unroll
            for (int i = 0; i < 5; ++i) acc[g][i] = f32x4{0, 0, 0, 0};
        PIN5(acc[0]); PIN5(acc[1]); PIN5(acc[2]); PIN5(acc[3]);
        u32x4 id[3];
        {
            const unsigned one = (m & 1) ? 0x3f800000u : 0x3f80u;
#pragma unroll
            for (int r = 0; r < 4; ++r) {
                const int k2 = 4 * g4 + r;
                id[0][r] = k2 == (m >> 1) ? one : 0u;
                id[1][r] = k2 == (m >> 1) + 8 ? one : 0u;
                id[2][r] = k2 == (m >> 1) + 8 * wn ? one : 0u;
            }
            asm volatile("" : "+v"(id[0]), "+v"(id[1]), "+v"(id[2]));
        }
        const int n = lng ? NL : NS;
        unsigned so = ((96 - n) % 3) * P1_SLOT;
        const unsigned rowb = base + so + P1_AB + (8 * g4 + q) * 320 + (p >> 1) * 16 + (p & 1) * 8, sw = (g4 & 1) * 32;
        P1Addr pa = {o0 + so + wm * 8192, o1 + so + wm * 8192, rowb + wn * 192 + sw, rowb + wn * 192 - sw, rowb + (((4 + wn) * 32) ^ sw)};
        P1Frags f0, f1;
        wg_barrier();
#pragma unroll
        for (int j = 0; j < 14; ++j) p1_rd<0>(j, f0, pa);
        int d = (so == 2 * P1_SLOT) ? -2 * P1_SLOT : P1_SLOT;
#pragma unroll 1
        for (int s = n - 2; s > 1; --s) {
            wait_lds<0>();
            p1_half<1>(acc, f0, f1, pa, [&](int k) { p1_move(k, pa, d); });
            wg_barrier();
            p1_half<0>(acc, f1, f0, pa, [&](int k) {
                if (k == 14) {
                    so += d;
                    d = (so == 2 * P1_SLOT) ? -2 * P1_SLOT : P1_SLOT;
                    asm volatile("" : "+s"(d), "+s"(so));
                }
            });
        }
        const unsigned sb = (4 * tb + 2 * wm) * TGS + nj * 5 * 1024;
        u32x4 pt[4][3];
        rsrc_t rpt = mk_rsrc(ws + PQ_HALF, lng ? PQ_HALF : 0);
        const unsigned v16 = wn * 3072 + lane * 16, vq = 2048 + lane * 16;
        unsigned sp[4];
#pragma unroll
        for (int g = 0; g < 4; ++g) sp[g] = sb + (g >> 1) * TGS + (g & 1) * 20 * 1024;
        auto ldp = [&](int k) {
            const int j = k / 4;
            if (k % 4 || j >= 12) return;
            if (j % 3 == 0) GLD(pt[j / 3][0], v16, rpt, sp[j / 3], 0);
            if (j % 3 == 1) GLD(pt[j / 3][1], v16, rpt, sp[j / 3], 1024);
            if (j % 3 == 2) GLD(pt[j / 3][2], vq, rpt, sp[j / 3], 0);
        };
        unsigned v8 = 2048 + wn * 512 + (g4 >> 1) * 256 + m * 16 + (g4 & 1) * 8;
        unsigned neg = lng ? 0x80008000u : 0u, dst = sb + (lng ? 0 : PQ_HALF);
        asm volatile("" : "+v"(v8));
        asm volatile("" : "+s"(neg), "+s"(dst));
        wait_lds<0>();
        p1_half<1>(acc, f0, f1, pa, [&](int k) { p1_move(k, pa, P1_SLOT); });
        wg_barrier();
        p1_half<0>(acc, f1, f0, pa, [&](int k) { ldp(k); });
        wait_lds<0>();
        p1_half<1>(acc, f0, f1, pa, [&](int k) { p1_move(k, pa, P1_SLOT); ldp(k + 20); });
        wg_barrier();
        p1_half<0>(acc, f1, f0, pa, [&](int k) { ldp(k + 40); });
        wait_lds<0>();
        p1_half<1>(acc, f0, f1, pa, [&](int) {});
        wait_lds<0>();
        __builtin_amdgcn_sched_barrier(0);
        asm volatile("s_waitcnt vmcnt(0)" : "+v"(pt[0][0]), "+v"(pt[0][1]), "+v"(pt[0][2]), "+v"(pt[1][0]), "+v"(pt[1][1]), "+v"(pt[1][2]),
                     "+v"(pt[2][0]), "+v"(pt[2][1]), "+v"(pt[2][2]), "+v"(pt[3][0]), "+v"(pt[3][1]), "+v"(pt[3][2]) :: "memory");
        unsigned pk[10];
        auto epi = [&](int g, int j) {
            const unsigned sg = (g & 1) ? neg : 0u, so2 = dst + (g >> 1) * TGS + (g & 1) * 20 * 1024;
#pragma unroll
            for (int c = 3 * j; c < 3 * j + 3; ++c)
                if (c < 10) pk[c] = pack2(acc[g][c >> 1][2 * (c & 1)], acc[g][c >> 1][2 * (c & 1) + 1]);
            if (j == 3) { swap32(pk[0], pk[2]); swap32(pk[1], pk[3]); }
            if (j == 4) { swap32(pk[4], pk[6]); swap32(pk[5], pk[7]); }
            if (j == 5) { swap16(pk[0], pk[2]); swap16(pk[1], pk[3]); }
            if (j == 6) { swap16(pk[4], pk[6]); swap16(pk[5], pk[7]); }
            if (j == 7) __builtin_amdgcn_raw_buffer_store_b128(u32x4{pk[0] ^ sg, pk[1] ^ sg, pk[2] ^ sg, pk[3] ^ sg}, rpq, v16, so2, 16);
            if (j == 8) __builtin_amdgcn_raw_buffer_store_b128(u32x4{pk[4] ^ sg, pk[5] ^ sg, pk[6] ^ sg, pk[7] ^ sg}, rpq, v16 + 1024, so2, 16);
            if (j == 7 || j == 8) asm volatile("s_nop 1" ::: "memory");
            if (j == 9) __builtin_amdgcn_raw_buffer_store_b64(u32x2{pk[8] ^ sg, pk[9] ^ sg}, rpq, v8, so2, 16);
        };
#pragma unroll
        for (int k = 0; k < 52; ++k) {
            if (k < 40) {
                const int g = k / 10, i = k % 5;
                const u32x4 a = (k % 10 < 5) ? cat(f1.x[i]) : id[i == 4 ? 2 : i & 1];
                const u32x4 b = (k % 10 < 5) ? f1.c[g] : pt[g][i >> 1];
                acc[g][i] = __builtin_amdgcn_mfma_f32_16x16x32_bf16(__builtin_bit_cast(bf16x8, a), __builtin_bit_cast(bf16x8, b), acc[g][i], 0, 0, 0);
            }
            if (k >= 12) epi((k - 12) / 10, (k - 12) % 10);
            __builtin_amdgcn_sched_barrier(0);
        }
        wg_barrier();
    }
    unsigned q0 = o0 + 10 * W_SLOT, q1 = o1 + 10 * W_SLOT;
#pragma unroll
    for (int u = 0; u < 4; ++u) {
#pragma unroll
        for (int j = 0; j < 5; ++j) { RD128(wf[u].w[0][j], q0, (u + 1) * W_SLOT + j * 2048); RD128(wf[u].w[1][j], q1, (u + 1) * W_SLOT + j * 2048); }
        wait_lds<0>();
    }
    f32x4 acc[5];
#pragma unroll
    for (int j = 0; j < 5; ++j) acc[j] = f32x4{0, 0, 0, 0};
    PIN5(acc);
    const int tg = tb * 4 + w, et = hi * 4 + nj;
    const unsigned va = lane * 16;
    unsigned sab[4];
#pragma unroll
    for (int b = 0; b < 4; ++b) sab[b] = tg * TGS + (((tb >> 1) + b) & 3) * 10240;
    u64 tag = epoch + 1;
    char* mine = ws + FLAG_OFF + (tb * 8 + hi * 4) * 128 + nj * (lng ? 8 : 128);
    const char* theirs = ws + FLAG_OFF + (tb * 8 + ((tb & 1) ^ 1) * 4) * 128 + (lane & 3) * 8;
    asm volatile("" : "+s"(sab[0]), "+s"(sab[1]), "+s"(sab[2]), "+s"(sab[3]));
    asm volatile("" : "+v"(tag), "+v"(mine), "+v"(theirs));
    wait_vm<0>();
    wg_barrier();
    if (w == 1 && lane == 0) asm volatile("global_atomic_swap_x2 %0, %1, off" :: "v"(mine), "v"(tag) : "memory");
    if (w == 0 && lane < 4) poll(theirs, tag);
    wg_barrier();
    constexpr int AD = 6, A0 = 4;
    u32x4 a[2 * AD];
    auto lda = [&](int u) {
        const unsigned sa = sab[u / 5] + (u % 5) * 2048;
        a[2 * (u % AD)] = __builtin_amdgcn_raw_buffer_load_b128(rpq, va, sa, 16);
        a[2 * (u % AD) + 1] = __builtin_amdgcn_raw_buffer_load_b128(rpq, va + 1024, sa, 16);
    };
#pragma unroll
    for (int u = 0; u < A0; ++u) lda(u);
    if (et == 0 && w == 0 && lane == 0)
        __hip_atomic_store((GAS u64*)(ws + EPOCH_OFF + tb * 128), tag, __ATOMIC_RELAXED, __HIP_MEMORY_SCOPE_AGENT);
#pragma unroll
    for (int u = 0; u < 20; ++u) {
        if (u < 19) wait_lds<10>(); else wait_lds<0>();
        if (u == 1) { VADD(q0, -10 * W_SLOT); VADD(q1, -10 * W_SLOT); }
        if (u == 7 || u == 13) { VADD(q0, 6 * W_SLOT); VADD(q1, 6 * W_SLOT); }
#pragma unroll
        for (int i = 0; i < 2; ++i)
#pragma unroll
            for (int j = 0; j < 5; ++j) {
                MFMA(acc[j], wf[u & 3].w[i][j], a[2 * (u % AD) + i]);
                if (u < 16) RE128(wf[u & 3].w[i][j], (i ? q1 : q0), (u == 0 ? 5 : (u - 1) % 6) * W_SLOT + j * 2048);
            }
        const int h0 = A0 + 2 * u < u + AD ? A0 + 2 * u : u + AD, h1 = A0 + 2 * u + 2 < u + 1 + AD ? A0 + 2 * u + 2 : u + 1 + AD;
#pragma unroll
        for (int v = h0; v < h1; ++v) if (v < 20) lda(v);
    }
    asm volatile("s_nop 7\n\ts_nop 7" : "+v"(acc[0]), "+v"(acc[1]), "+v"(acc[2]), "+v"(acc[3]), "+v"(acc[4]));
    rsrc_t ry = mk_rsrc(y, S_ * D_ * 2);
    const unsigned vy = m * (D_ * 2) + g4 * 16, sy = tg * (16 * D_ * 2) + et * 160;
    __builtin_amdgcn_raw_buffer_store_b128(pack_pair(acc[0], acc[1]), ry, vy, sy, 0);
    __builtin_amdgcn_raw_buffer_store_b128(pack_pair(acc[2], acc[3]), ry, vy + 64, sy, 0);
    asm volatile("s_nop 1" ::: "memory");
    __builtin_amdgcn_raw_buffer_store_b64(u32x2{pack2(acc[4][0], acc[4][1]), pack2(acc[4][2], acc[4][3])}, ry,
                                          m * (D_ * 2) + 128 + g4 * 8, sy, 0);
}
#endif

__global__ __launch_bounds__(384, 2) void fnet_fused(const void* __restrict__ x, const void* __restrict__ cr,
                                                     const void* __restrict__ ci, const void* __restrict__ wre,
                                                     const void* __restrict__ wim, char* ws, char* __restrict__ y) {
#if defined(__HIP_DEVICE_COMPILE__)
    __shared__ __attribute__((aligned(1024))) char smem_[163840];
    LAS char* smem = (LAS char*)smem_;
    const int xcd = blockIdx.x & 7, idx = blockIdx.x >> 3;
    const int hi = xcd & 1, nj = idx & 3, tb = 8 * (xcd >> 1) + (idx >> 2), lng = hi ^ (tb & 1);
    const int lane = threadIdx.x & 63;
    const int wid = __builtin_amdgcn_readfirstlane(threadIdx.x >> 6);
    if (__builtin_expect(wid < 4, 0)) compute(smem, ws, y, tb, nj, hi, lng, wid, lane);
    else loader(smem, x, cr, ci, wre, wim, ws, tb, nj, hi, lng, wid - 4, lane);
#endif
}

extern "C" void kernel_launch(void* const* d_in, const int* in_sizes, int n_in, void* d_out, int out_size,
                              void* d_ws, size_t ws_size, hipStream_t stream) {
    (void)in_sizes; (void)n_in; (void)out_size; (void)ws_size;
    fnet_fused<<<256, 384, 0, stream>>>(d_in[0], d_in[1], d_in[2], d_in[3], d_in[4], (char*)d_ws, (char*)d_out);
}
```

```cpp
#include <hip/hip_runtime.h>
#include <hip/hip_bf16.h>
#include <type_traits>

typedef __attribute__((ext_vector_type(2))) __bf16 bf16x2;
typedef __attribute__((ext_vector_type(2))) float f32x2;
typedef __attribute__((ext_vector_type(4))) float f32x4;
typedef __attribute__((ext_vector_type(4))) unsigned u32x4;
typedef __attribute__((ext_vector_type(2))) unsigned u32x2;
typedef unsigned long long u64;
#define LAS __attribute__((address_space(3)))
#define GAS __attribute__((address_space(1)))

constexpr int S_ = 2048, D_ = 640;
constexpr int TGS = 40 * 1024 + 128;
constexpr int PQ_HALF = 128 * TGS, PQ_BYTES = 2 * PQ_HALF;
constexpr int FLAG_OFF = PQ_BYTES;
constexpr int EPOCH_OFF = FLAG_OFF + 256 * 128;
constexpr int NS = 13, NL = 32 - NS;
constexpr int P1_AB = 16384, X_ODD = 10248, P1_SLOT = 36880, W_SLOT = 10240;
constexpr int SPIN_CAP = 1 << 20;

#if defined(__HIP_DEVICE_COMPILE__)
typedef __amdgpu_buffer_rsrc_t rsrc_t;
template <int N> __device__ __forceinline__ void wait_vm() { asm volatile("s_waitcnt vmcnt(%0)" ::"i"(N) : "memory"); }
template <int N> __device__ __forceinline__ void wait_lds() { asm volatile("s_waitcnt lgkmcnt(%0)" ::"i"(N) : "memory"); }
__device__ __forceinline__ void wg_barrier() { asm volatile("s_waitcnt lgkmcnt(0)\n\ts_barrier" ::: "memory"); }
__device__ __forceinline__ rsrc_t mk_rsrc(const void* p, unsigned bytes) {
    return __builtin_amdgcn_make_buffer_rsrc((void*)p, 0, bytes, 0x00020000);
}
__device__ __forceinline__ void dma16(rsrc_t r, LAS char* dst, unsigned voff, unsigned soff) {
    __builtin_amdgcn_raw_ptr_buffer_load_lds(r, (LAS void*)dst, 16, voff, soff, 0, 0);
}
__device__ __forceinline__ unsigned pack2(float a, float b) {
    f32x2 v = {a, b};
    return __builtin_bit_cast(unsigned, __builtin_convertvector(v, bf16x2));
}
__device__ __forceinline__ unsigned pack2n(float a, float b) {
    unsigned r;
    asm("v_cvt_pk_bf16_f32 %0, -%1, -%2" : "=v"(r) : "v"(a), "v"(b));
    return r;
}
__device__ __forceinline__ void swap16(unsigned& a, unsigned& b) { auto r = __builtin_amdgcn_permlane16_swap(a, b, false, false); a = r[0]; b = r[1]; }
__device__ __forceinline__ u32x4 pack_pair(f32x4 a, f32x4 b) {
    return u32x4{pack2(a[0], a[1]), pack2(a[2], a[3]), pack2(b[0], b[1]), pack2(b[2], b[3])};
}
template <int I, int N, class F> __device__ __forceinline__ void static_for(F&& f) {
    if constexpr (I < N) { f(std::integral_constant<int, I>{}); static_for<I + 1, N>(f); }
}
#define PIN5(a) asm volatile("" : "+v"(a[0]), "+v"(a[1]), "+v"(a[2]), "+v"(a[3]), "+v"(a[4]))
#define MFMA(acc, a, b) asm volatile("v_mfma_f32_16x16x32_bf16 %0, %1, %2, %0" : "+v"(acc) : "v"(a), "v"(b))
#define RD128(d, ad, off) asm volatile("ds_read_b128 %0, %1 offset:%2" : "=v"(d) : "v"(ad), "i"(off))
#define RE128(d, ad, off) asm volatile("ds_read_b128 %0, %1 offset:%2" : "+v"(d) : "v"(ad), "i"(off))
#define RDTR(d, ad, off) asm volatile("ds_read_b64_tr_b16 %0, %1 offset:%2" : "=v"(d) : "v"(ad), "i"(off))
#define GLD(d, vo, rs, so, off) asm volatile("buffer_load_dwordx4 %0, %1, %2, %3 offen offset:%4 sc1" : "=v"(d) : "v"(vo), "s"(rs), "s"(so), "i"(off) : "memory")
#define VADD(r, d) asm volatile("v_add_u32 %0, %1, %0" : "+v"(r) : "s"(d))
#define PEEK(v, p, z) asm volatile("global_atomic_or_x2 %0, %1, %2, off sc0" : "+&v"(v) : "v"(p), "v"(z) : "memory")

__device__ __forceinline__ u64 ld_agent(const char* p) {
    return __hip_atomic_load((GAS u64*)p, __ATOMIC_RELAXED, __HIP_MEMORY_SCOPE_AGENT);
}
__device__ __forceinline__ void poll(const char* p, u64 tag) {
    u64 v = 0, z = 0;
#pragma unroll 1
    for (int spin = 0; spin < SPIN_CAP; ++spin) {
        PEEK(v, p, z);
        asm volatile("s_waitcnt vmcnt(0)" : "+v"(v) :: "memory");
        if (__all(v == tag)) break;
    }
}

__device__ __forceinline__ void loader(LAS char* smem, const void* x, const void* cr, const void* ci, const void* wre,
                                       const void* wim, char* ws, int tb, int nj, int hi, int lng, int lw, int lane) {
    const int row = 8 * lw + (lane >> 3);
    const unsigned swz = ((lane & 7) ^ ((row >> 1) & 7)) << 4;
    unsigned voA = row * (S_ * 2) + swz, voB[5];
    const int gr = (tb >> 1) & 3, flip = hi ? 31 : 0, gj = gr ^ nj;
    int kap = gj ^ flip;
    unsigned lds = lng ? (96 - NL) % 3 * P1_SLOT : (96 - NS) % 3 * P1_SLOT;
    asm volatile("" : "+v"(voA));
    asm volatile("" : "+s"(kap), "+s"(lds));
    __builtin_amdgcn_sched_barrier(0);
    rsrc_t rr = mk_rsrc((const char*)cr + (size_t)tb * (64 * S_ * 2), 64 * S_ * 2);
    rsrc_t ri = mk_rsrc((const char*)ci + (size_t)tb * (64 * S_ * 2), 64 * S_ * 2);
    rsrc_t rx = mk_rsrc((const char*)x + nj * 320, S_ * D_ * 2 - nj * 320);
    const int n = lng ? NL : NS, rot = (((gr + nj) & 3) * (n - 12)) >> 2;
    auto kappa = [&](int s) {
        const int a = s + rot, b = a < n ? a : a + 12 - n;
        return (s < 12 ? s ^ gj : b) ^ flip;
    };
    auto issue_c = [&](int kap, unsigned lds) {
        LAS char* sb = smem + lds + lw * 1024;
#pragma unroll
        for (int g = 0; g < 8; ++g) dma16((g & 1) ? ri : rr, sb + g * 2048, voA, kap * 128 + (g >> 1) * (16 * S_ * 2));
    };
    auto issue_x = [&](int kap, unsigned lds) {
        LAS char* sb = smem + lds + P1_AB + lw * X_ODD;
#pragma unroll
        for (int b = 0; b < 10; ++b)
            dma16(rx, sb + b * 1024, voB[b % 5], kap * (64 * D_ * 2) + (b / 5) * (32 * D_ * 2));
    };
    auto next_slot = [&](unsigned lds) { return lds == 2 * P1_SLOT ? 0u : lds + P1_SLOT; };
    issue_c(kap, lds);
    __builtin_amdgcn_sched_barrier(0);
#pragma unroll
    for (int b = 0; b < 5; ++b) {
        const unsigned c = b * 64 + lane, kr = __umul24(c, 3277) >> 16;
        voB[b] = __umul24(kr, D_ * 2 - 320) + __umul24(kr & 8, D_ * 2) + c * 16 + lw * (8 * D_ * 2);
    }
    issue_x(kap, lds);
    lds = next_slot(lds);
    issue_c(kap ^ 1, lds); issue_x(kap ^ 1, lds);
    lds = next_slot(lds);
    const u64 tag = ld_agent(ws + EPOCH_OFF + tb * 128) + 1;
    const char* pre = (const char*)wre + (hi * 4 + nj) * 80 * (D_ * 2);
    const char* pim = (const char*)wim + (hi * 4 + nj) * 80 * (D_ * 2);
    const unsigned voW = (row + (row & 12)) * (D_ * 2) + swz, voW4 = row * (D_ * 2) + swz;
    auto issue_w = [&](int u0, int u1) {
#pragma unroll 1
        for (int u = u0; u < u1; ++u) {
            int sg = u + 5 * gr;
            sg = sg >= 20 ? sg - 20 : sg;
            rsrc_t r = mk_rsrc(sg < 10 ? pre : pim, 80 * D_ * 2);
            const int ks = sg < 10 ? sg : sg - 10;
            LAS char* sb = smem + ((u + 11) & 15) * W_SLOT + lw * 1024;
#pragma unroll
            for (int j = 0; j < 5; ++j) dma16(r, sb + j * 2048, j < 4 ? voW : voW4, (32 * (j >> 1) + 4 * (j & 1)) * (D_ * 2) + ks * 128);
        }
    };
    if (!lng) { issue_w(0, 5); wait_vm<18 + 25>(); } else wait_vm<18>();
    const unsigned ride = lng ? 0x1198 : 0u;
    int un = 0;
    kap ^= 2;
#pragma unroll 1
    for (int s = 0; s < n - 3; ++s) {
        wg_barrier();
        __builtin_amdgcn_sched_barrier(0);
        if (__builtin_expect((ride >> s) & 1, 0)) { issue_w(un, un + 1); ++un; }
        issue_c(kap, lds);
        __builtin_amdgcn_sched_barrier(0);
        int kn = kappa(s + 3);
        unsigned ln = next_slot(lds);
        asm volatile("" : "+s"(kn), "+s"(ln));
        __builtin_amdgcn_sched_barrier(0);
        issue_x(kap, lds);
        __builtin_amdgcn_sched_barrier(0);
        kap = kn; lds = ln;
        wait_vm<18>();
    }
    const char* pf = ws + FLAG_OFF + (tb * 8 + (hi ^ 1) * 4 + nj) * 128;
    const bool peek = lng && lw == 0 && lane == 0;
    u64 seen = 0, z = 0;
    wg_barrier();
    issue_c(kap, lds);
    if (peek) PEEK(seen, pf, z);
    issue_x(kap, lds);
    if (lng) {
        asm volatile("s_waitcnt vmcnt(10)" : "+v"(seen) :: "memory");
        if (peek && seen != tag) poll(pf, tag);
        wg_barrier();
        __builtin_amdgcn_s_sleep(4);
        issue_w(5, 8);
        wait_vm<15>(); wg_barrier();
        issue_w(8, 12);
        wg_barrier();
        issue_w(12, 15);
        wg_barrier();
    } else {
        wait_vm<18>(); wg_barrier();
        wait_vm<0>(); wg_barrier();
        wg_barrier();
        wg_barrier();
        __builtin_amdgcn_s_sleep(2);
        issue_w(5, 16);
    }
    __builtin_amdgcn_s_sleep(3);
    issue_w(lng ? 15 : 16, 20);
    wait_vm<0>(); wg_barrier();
}

struct XF { u32x2 lo, hi; };
struct P1Frags { u32x4 c[4]; XF x[5]; };
struct P1Addr { unsigned a0, a1, b0, b4; };
struct WF { u32x4 w[2][5]; };
__device__ __forceinline__ u32x4 cat(const XF& x) { return u32x4{x.lo[0], x.lo[1], x.hi[0], x.hi[1]}; }

template <int KS> __device__ __forceinline__ void p1_rd(int j, P1Frags& n, const P1Addr& p) {
    constexpr int KO = KS * 5120;
    const unsigned a = KS ? p.a1 : p.a0;
    if (j == 0) RDTR(n.x[0].lo, p.b0, KO);
    if (j == 1) RDTR(n.x[0].hi, p.b0, KO + 1280);
    if (j == 2) RD128(n.c[0], a, 0);
    if (j == 3) RD128(n.c[1], a, 2048);
    if (j == 4) RD128(n.c[2], a, 4096);
    if (j == 5) RD128(n.c[3], a, 6144);
    if (j == 6) RDTR(n.x[1].lo, p.b0, KO + 8);
    if (j == 7) RDTR(n.x[1].hi, p.b0, KO + 8 + 1280);
    if (j == 8) RDTR(n.x[2].lo, p.b0, KO + 64);
    if (j == 9) RDTR(n.x[2].hi, p.b0, KO + 64 + 1280);
    if (j == 10) RDTR(n.x[3].lo, p.b0, KO + 72);
    if (j == 11) RDTR(n.x[3].hi, p.b0, KO + 72 + 1280);
    if (j == 12) RDTR(n.x[4].lo, p.b4, KO);
    if (j == 13) RDTR(n.x[4].hi, p.b4, KO + 1280);
}
template <int KS, class H>
__device__ __forceinline__ void p1_half(f32x4 (&acc)[4][5], const P1Frags& f, P1Frags& n, const P1Addr& p, H hook) {
#pragma unroll
    for (int k = 0; k < 20; ++k) {
        u32x4 xx = cat(f.x[k >> 2]);
        MFMA(acc[k & 3][k >> 2], xx, f.c[k & 3]);
        p1_rd<KS>(k, n, p);
        hook(k);
    }
}
__device__ __forceinline__ void p1_move(int k, P1Addr& p, int d) {
    if (k == 14) { VADD(p.a0, d); VADD(p.b0, d); }
    if (k == 15) { VADD(p.a1, d); VADD(p.b4, d); }
}

__device__ __forceinline__ void compute(LAS char* smem, char* ws, char* y, int tb, int nj, int hi, int lng, int w, int lane
) {
    const u64 epoch = ld_agent(ws + EPOCH_OFF + tb * 128);
    const unsigned base = (unsigned)(size_t)smem;
    const int wm = w >> 1, wn = w & 1;
    const int m = lane & 15, g4 = lane >> 4, q = (lane & 15) >> 2, p = lane & 3;
    const unsigned o0 = base + m * 128 + ((g4 ^ ((m >> 1) & 7)) << 4), o1 = o0 ^ 64;
    rsrc_t rpq = mk_rsrc(ws, PQ_BYTES);
    WF wf[4];
    unsigned q0 = o0 + 10 * W_SLOT, q1 = o1 + 10 * W_SLOT;
    {
        f32x4 acc[4][5];
#pragma unroll
        for (int g = 0; g < 4; ++g)
#pragma unroll
            for (int i = 0; i < 5; ++i) acc[g][i] = f32x4{0, 0, 0, 0};
        PIN5(acc[0]); PIN5(acc[1]); PIN5(acc[2]); PIN5(acc[3]);
        u32x4 id[3];
        {
            const unsigned one = (m & 1) ? 0x3f800000u : 0x3f80u;
#pragma unroll
            for (int r = 0; r < 4; ++r) {
                id[0][r] = g4 == q && r == (p >> 1) ? one : 0u;
                id[1][r] = g4 == q && r == 2 + (p >> 1) ? one : 0u;
                id[2][r] = 4 * g4 + r == (m >> 1) + 8 * wn ? one : 0u;
            }
            asm volatile("" : "+v"(id[0]), "+v"(id[1]), "+v"(id[2]));
        }
        const int n = lng ? NL : NS;
        unsigned so = lng ? (96 - NL) % 3 * P1_SLOT : (96 - NS) % 3 * P1_SLOT;
        const unsigned rowb = base + so + P1_AB + (g4 & 1) * X_ODD + ((g4 >> 1) * 8 + q) * 320;
        P1Addr pa = {o0 + so + wm * 8192, o1 + so + wm * 8192, rowb + wn * 192 + p * 16, rowb + 128 + wn * 32 + p * 8};
        P1Frags f0, f1;
        wg_barrier();
#pragma unroll
        for (int j = 0; j < 14; ++j) p1_rd<0>(j, f0, pa);
        int d = (so == 2 * P1_SLOT) ? -2 * P1_SLOT : P1_SLOT;
#pragma unroll 1
        for (int s = n - 2; s > 1; --s) {
            wait_lds<0>();
            p1_half<1>(acc, f0, f1, pa, [&](int k) { p1_move(k, pa, d); });
            wg_barrier();
            p1_half<0>(acc, f1, f0, pa, [&](int k) {
                if (k == 14) {
                    so += d;
                    d = (so == 2 * P1_SLOT) ? -2 * P1_SLOT : P1_SLOT;
                    asm volatile("" : "+s"(d), "+s"(so));
                }
            });
        }
        const unsigned sb = (4 * tb + 2 * wm) * TGS + nj * 5 * 1024;
        u32x4 pt[4][3];
        rsrc_t rpt = mk_rsrc(ws + PQ_HALF, lng ? PQ_HALF : 0);
        const unsigned v16 = wn * 3072 + lane * 16, vq = 2048 + lane * 16;
        unsigned sp[4];
#pragma unroll
        for (int o = 0; o < 4; ++o) sp[o] = sb + (o & 1) * TGS + (o >> 1) * 20 * 1024;
        auto ldp = [&](int k) {
            const int j = k / 3;
            if (k % 3 || j >= 12) return;
            if (j % 3 == 0) GLD(pt[j / 3][0], v16, rpt, sp[j / 3], 0);
            if (j % 3 == 1) GLD(pt[j / 3][1], v16, rpt, sp[j / 3], 1024);
            if (j % 3 == 2) GLD(pt[j / 3][2], vq, rpt, sp[j / 3], 0);
        };
        unsigned dst = sb + (lng ? 0 : PQ_HALF), vh = (g4 & 1) * TGS + 2048 + wn * 512 + (g4 >> 1) * 256 + m * 16;
        asm volatile("" : "+v"(vh));
        asm volatile("" : "+s"(dst));
        wait_lds<0>();
        p1_half<1>(acc, f0, f1, pa, [&](int k) { p1_move(k, pa, P1_SLOT); });
        wg_barrier();
        p1_half<0>(acc, f1, f0, pa, [&](int k) { ldp(k); });
        wait_lds<0>();
        p1_half<1>(acc, f0, f1, pa, [&](int k) { p1_move(k, pa, P1_SLOT); ldp(k + 20); });
        wg_barrier();
        p1_half<0>(acc, f1, f0, pa, [&](int k) { ldp(k + 40); });
        wait_lds<0>();
        p1_half<1>(acc, f0, f1, pa, [&](int) {});
        wait_lds<0>();
        __builtin_amdgcn_sched_barrier(0);
        auto rdw = [&](int u, int j) {
            if (j < 5) RD128(wf[u].w[0][j % 5], q0, (u + 1) * W_SLOT + (j % 5) * 2048);
            else RD128(wf[u].w[1][j % 5], q1, (u + 1) * W_SLOT + (j % 5) * 2048);
        };
        unsigned pk[8], ph[2][2];
        static_for<0, 48>([&](auto kc) {
            constexpr int k = kc, o = k / 10, g = (o & 1) * 2 + (o >> 1), i = k % 5;
            if constexpr (k < 40 && k % 10 == 5)
                asm volatile("s_waitcnt vmcnt(%3)" : "+v"(pt[o][0]), "+v"(pt[o][1]), "+v"(pt[o][2]) : "i"(k < 10 ? 9 : k < 20 ? 7 : 6) : "memory");
            if constexpr (k < 40) {
                const u32x4 a = (k % 10 < 5) ? cat(f1.x[i]) : id[i == 4 ? 2 : i & 1];
                const u32x4 b = (k % 10 < 5) ? f1.c[g] : pt[o][i >> 1];
                MFMA(acc[g][i], a, b);
            }
            if constexpr (k >= 20 && k < 23) {
                id[k - 20] ^= u32x4{0x80008000u, 0x80008000u, 0x80008000u, 0x80008000u};
                asm volatile("" : "+v"(id[k - 20]));
            }
            if constexpr (k >= 12 && (k - 12) % 10 < 6) {
                constexpr int e = (k - 12) / 10, h = (e & 1) * 2 + (e >> 1), j = (k - 12) % 10;
                if constexpr (j == 0)
                    asm volatile("s_nop 1" : "+v"(acc[h][0]), "+v"(acc[h][1]), "+v"(acc[h][2]), "+v"(acc[h][3]), "+v"(acc[h][4]));
                if constexpr (j < 5) {
                    unsigned& r0 = j < 4 ? pk[2 * (j & 3)] : ph[e & 1][0];
                    unsigned& r1 = j < 4 ? pk[2 * (j & 3) + 1] : ph[e & 1][1];
                    if constexpr (h & 1) { r0 = pack2n(acc[h][j][0], acc[h][j][1]); r1 = pack2n(acc[h][j][2], acc[h][j][3]); }
                    else { r0 = pack2(acc[h][j][0], acc[h][j][1]); r1 = pack2(acc[h][j][2], acc[h][j][3]); }
                }
                const unsigned so2 = dst + (e & 1) * TGS + (e >> 1) * 20 * 1024;
                if constexpr (j == 2) __builtin_amdgcn_raw_buffer_store_b128(u32x4{pk[0], pk[1], pk[2], pk[3]}, rpq, v16, so2, 16);
                if constexpr (j == 4) __builtin_amdgcn_raw_buffer_store_b128(u32x4{pk[4], pk[5], pk[6], pk[7]}, rpq, v16 + 1024, so2, 16);
                if constexpr (j == 5 && (e & 1)) {
                    swap16(ph[0][0], ph[1][0]); swap16(ph[0][1], ph[1][1]);
                    __builtin_amdgcn_raw_buffer_store_b128(u32x4{ph[0][0], ph[0][1], ph[1][0], ph[1][1]}, rpq, vh, dst + (e >> 1) * 20 * 1024, 16);
                }
                if constexpr (j == 2 || j >= 4) asm volatile("s_nop 1" ::: "memory");
            }
            if constexpr (k < 10) rdw(0, k);
            if constexpr (k >= 24 && k < 24 + 10) rdw(1, k - 24);
            __builtin_amdgcn_sched_barrier(0);
        });
        static_for<0, 20>([&](auto kc) { rdw(2 + kc / 10, kc % 10); if constexpr (kc == 9) wait_lds<4>(); });
        wg_barrier();
    }
    f32x4 acc[5];
#pragma unroll
    for (int j = 0; j < 5; ++j) acc[j] = f32x4{0, 0, 0, 0};
    PIN5(acc);
    const int tg = tb * 4 + w, et = hi * 4 + nj;
    const unsigned va = lane * 16;
    unsigned sab[4];
#pragma unroll
    for (int b = 0; b < 4; ++b) sab[b] = tg * TGS + (((tb >> 1) + b) & 3) * 10240;
    u64 tag = epoch + 1;
    char* mine = ws + FLAG_OFF + (tb * 8 + hi * 4) * 128 + nj * (lng ? 8 : 128);
    const char* theirs = ws + FLAG_OFF + (tb * 8 + ((tb & 1) ^ 1) * 4) * 128 + (lane & 3) * 8;
    asm volatile("" : "+s"(sab[0]), "+s"(sab[1]), "+s"(sab[2]), "+s"(sab[3]));
    asm volatile("" : "+v"(tag), "+v"(mine), "+v"(theirs));
    wait_vm<0>();
    wg_barrier();
    if (w == 1 && lane == 0) asm volatile("global_atomic_swap_x2 %0, %1, off" :: "v"(mine), "v"(tag) : "memory");
    if (w == 0 && lane < 4) poll(theirs, tag);
    wg_barrier();
    constexpr int AD = 6, A0 = 4;
    u32x4 a[2 * AD];
    auto lda = [&](int u) {
        const unsigned sa = sab[u / 5] + (u % 5) * 2048;
        a[2 * (u % AD)] = __builtin_amdgcn_raw_buffer_load_b128(rpq, va, sa, 16);
        a[2 * (u % AD) + 1] = __builtin_amdgcn_raw_buffer_load_b128(rpq, va + 1024, sa, 16);
    };
#pragma unroll
    for (int u = 0; u < A0; ++u) lda(u);
    if (et == 0 && w == 0 && lane == 0)
        __hip_atomic_store((GAS u64*)(ws + EPOCH_OFF + tb * 128), tag, __ATOMIC_RELAXED, __HIP_MEMORY_SCOPE_AGENT);
#pragma unroll
    for (int u = 0; u < 20; ++u) {
        if (u < 19) wait_lds<10>(); else wait_lds<0>();
        if (u == 1) { VADD(q0, -10 * W_SLOT); VADD(q1, -10 * W_SLOT); }
        if (u == 7 || u == 13) { VADD(q0, 6 * W_SLOT); VADD(q1, 6 * W_SLOT); }
#pragma unroll
        for (int i = 0; i < 2; ++i)
#pragma unroll
            for (int j = 0; j < 5; ++j) {
                MFMA(acc[j], wf[u & 3].w[i][j], a[2 * (u % AD) + i]);
                if (u < 16) RE128(wf[u & 3].w[i][j], (i ? q1 : q0), (u == 0 ? 5 : (u - 1) % 6) * W_SLOT + j * 2048);
            }
        const int h0 = A0 + 2 * u < u + AD ? A0 + 2 * u : u + AD, h1 = A0 + 2 * u + 2 < u + 1 + AD ? A0 + 2 * u + 2 : u + 1 + AD;
#pragma unroll
        for (int v = h0; v < h1; ++v) if (v < 20) lda(v);
    }
    asm volatile("s_nop 7\n\ts_nop 7" : "+v"(acc[0]), "+v"(acc[1]), "+v"(acc[2]), "+v"(acc[3]), "+v"(acc[4]));
    rsrc_t ry = mk_rsrc(y, S_ * D_ * 2);
    const unsigned vy = m * (D_ * 2) + g4 * 16, sy = tg * (16 * D_ * 2) + et * 160;
    __builtin_amdgcn_raw_buffer_store_b128(pack_pair(acc[0], acc[1]), ry, vy, sy, 0);
    __builtin_amdgcn_raw_buffer_store_b128(pack_pair(acc[2], acc[3]), ry, vy + 64, sy, 0);
    asm volatile("s_nop 1" ::: "memory");
    __builtin_amdgcn_raw_buffer_store_b64(u32x2{pack2(acc[4][0], acc[4][1]), pack2(acc[4][2], acc[4][3])}, ry,
                                          m * (D_ * 2) + 128 + g4 * 8, sy, 0);
}
#endif

__global__ __launch_bounds__(384, 2) void fnet_fused(const void* __restrict__ x, const void* __restrict__ cr,
                                                     const void* __restrict__ ci, const void* __restrict__ wre,
                                                     const void* __restrict__ wim, char* ws, char* __restrict__ y) {
#if defined(__HIP_DEVICE_COMPILE__)
    __shared__ __attribute__((aligned(1024))) char smem_[163840];
    LAS char* smem = (LAS char*)smem_;
    const int xcd = blockIdx.x & 7, idx = blockIdx.x >> 3;
    const int hi = xcd & 1, nj = idx & 3, tb = 8 * (xcd >> 1) + (idx >> 2), lng = hi ^ (tb & 1);
    const int lane = threadIdx.x & 63;
    const int wid = __builtin_amdgcn_readfirstlane(threadIdx.x >> 6);
    if (__builtin_expect(wid < 4, 0)) compute(smem, ws, y, tb, nj, hi, lng, wid, lane);
    else loader(smem, x, cr, ci, wre, wim, ws, tb, nj, hi, lng, wid - 4, lane);
#endif
}

extern "C" void kernel_launch(void* const* d_in, const int* in_sizes, int n_in, void* d_out, int out_size,
                              void* d_ws, size_t ws_size, hipStream_t stream) {
    (void)in_sizes; (void)n_in; (void)out_size; (void)ws_size;
    fnet_fused<<<256, 384, 0, stream>>>(d_in[0], d_in[1], d_in[2], d_in[3], d_in[4], (char*)d_ws, (char*)d_out);
}
```

```cpp
#include <hip/hip_runtime.h>
#include <hip/hip_bf16.h>
#include <type_traits>

typedef __attribute__((ext_vector_type(2))) __bf16 bf16x2;
typedef __attribute__((ext_vector_type(2))) float f32x2;
typedef __attribute__((ext_vector_type(4))) float f32x4;
typedef __attribute__((ext_vector_type(4))) unsigned u32x4;
typedef __attribute__((ext_vector_type(2))) unsigned u32x2;
typedef unsigned long long u64;
#define LAS __attribute__((address_space(3)))
#define GAS __attribute__((address_space(1)))

constexpr int S_ = 2048, D_ = 640;
constexpr int TGS = 40 * 1024 + 128;
constexpr int PQ_HALF = 128 * TGS, PQ_BYTES = 2 * PQ_HALF;
constexpr int FLAG_OFF = PQ_BYTES;
constexpr int EPOCH_OFF = FLAG_OFF + 256 * 128;
constexpr int NS = 13, NL = 32 - NS;
constexpr int P1_AB = 16384, X_ODD = 10248, P1_SLOT = 36880, W_SLOT = 10240;
constexpr int SPIN_CAP = 1 << 20;

#if defined(__HIP_DEVICE_COMPILE__)
typedef __amdgpu_buffer_rsrc_t rsrc_t;
template <int N> __device__ __forceinline__ void wait_vm() { asm volatile("s_waitcnt vmcnt(%0)" ::"i"(N) : "memory"); }
template <int N> __device__ __forceinline__ void wait_lds() { asm volatile("s_waitcnt lgkmcnt(%0)" ::"i"(N) : "memory"); }
__device__ __forceinline__ void wg_barrier() { asm volatile("s_waitcnt lgkmcnt(0)\n\ts_barrier" ::: "memory"); }
__device__ __forceinline__ rsrc_t mk_rsrc(const void* p, unsigned bytes) {
    return __builtin_amdgcn_make_buffer_rsrc((void*)p, 0, bytes, 0x00020000);
}
__device__ __forceinline__ void dma16(rsrc_t r, LAS char* dst, unsigned voff, unsigned soff) {
    __builtin_amdgcn_raw_ptr_buffer_load_lds(r, (LAS void*)dst, 16, voff, soff, 0, 0);
}
__device__ __forceinline__ unsigned pack2(float a, float b) {
    f32x2 v = {a, b};
    return __builtin_bit_cast(unsigned, __builtin_convertvector(v, bf16x2));
}
__device__ __forceinline__ unsigned pack2n(float a, float b) {
    unsigned r;
    asm("v_cvt_pk_bf16_f32 %0, -%1, -%2" : "=v"(r) : "v"(a), "v"(b));
    return r;
}
__device__ __forceinline__ void swap16(unsigned& a, unsigned& b) { auto r = __builtin_amdgcn_permlane16_swap(a, b, false, false); a = r[0]; b = r[1]; }
__device__ __forceinline__ u32x4 pack_pair(f32x4 a, f32x4 b) {
    return u32x4{pack2(a[0], a[1]), pack2(a[2], a[3]), pack2(b[0], b[1]), pack2(b[2], b[3])};
}
template <int I, int N, class F> __device__ __forceinline__ void static_for(F&& f) {
    if constexpr (I < N) { f(std::integral_constant<int, I>{}); static_for<I + 1, N>(f); }
}
#define PIN5(a) asm volatile("" : "+v"(a[0]), "+v"(a[1]), "+v"(a[2]), "+v"(a[3]), "+v"(a[4]))
#define MFMA(acc, a, b) asm volatile("v_mfma_f32_16x16x32_bf16 %0, %1, %2, %0" : "+v"(acc) : "v"(a), "v"(b))
#define RD128(d, ad, off) asm volatile("ds_read_b128 %0, %1 offset:%2" : "=v"(d) : "v"(ad), "i"(off))
#define RE128(d, ad, off) asm volatile("ds_read_b128 %0, %1 offset:%2" : "+v"(d) : "v"(ad), "i"(off))
#define RDTR(d, ad, off) asm volatile("ds_read_b64_tr_b16 %0, %1 offset:%2" : "=v"(d) : "v"(ad), "i"(off))
#define GLD(d, vo, rs, so, off) asm volatile("buffer_load_dwordx4 %0, %1, %2, %3 offen offset:%4 sc1" : "=v"(d) : "v"(vo), "s"(rs), "s"(so), "i"(off) : "memory")
#define VADD(r, d) asm volatile("v_add_u32 %0, %1, %0" : "+v"(r) : "s"(d))
#define PEEK(v, p, z) asm volatile("global_atomic_or_x2 %0, %1, %2, off sc0" : "+&v"(v) : "v"(p), "v"(z) : "memory")

__device__ __forceinline__ u64 ld_agent(const char* p) {
    return __hip_atomic_load((GAS u64*)p, __ATOMIC_RELAXED, __HIP_MEMORY_SCOPE_AGENT);
}
__device__ __forceinline__ void poll(const char* p, u64 tag) {
    u64 v = 0, z = 0;
#pragma unroll 1
    for (int spin = 0; spin < SPIN_CAP; ++spin) {
        PEEK(v, p, z);
        asm volatile("s_waitcnt vmcnt(0)" : "+v"(v) :: "memory");
        if (__all(v == tag)) break;
    }
}

__device__ __forceinline__ void loader(LAS char* smem, const void* x, const void* cr, const void* ci, const void* wre,
                                       const void* wim, char* ws, int tb, int nj, int hi, int lng, int lw, int lane) {
    const int row = 8 * lw + (lane >> 3);
    const unsigned swz = ((lane & 7) ^ ((row >> 1) & 7)) << 4;
    unsigned voA = row * (S_ * 2) + swz, voB[5];
    const int gr = (tb >> 1) & 3, flip = (hi ? 31 : 0) ^ (tb & 8) * 2, gj = gr ^ nj;
    int kap = gj ^ flip;
    unsigned lds = lng ? (96 - NL) % 3 * P1_SLOT : (96 - NS) % 3 * P1_SLOT;
    asm volatile("" : "+v"(voA));
    asm volatile("" : "+s"(kap), "+s"(lds));
    __builtin_amdgcn_sched_barrier(0);
    rsrc_t rr = mk_rsrc((const char*)cr + (size_t)tb * (64 * S_ * 2), 64 * S_ * 2);
    rsrc_t ri = mk_rsrc((const char*)ci + (size_t)tb * (64 * S_ * 2), 64 * S_ * 2);
    rsrc_t rx = mk_rsrc((const char*)x + nj * 320, S_ * D_ * 2 - nj * 320);
    const int n = lng ? NL : NS, rot = (((gr + nj) & 3) * (n - 12)) >> 2;
    auto kappa = [&](int s) {
        const int a = s + rot, b = a < n ? a : a + 12 - n;
        return (s < 12 ? s ^ gj : b) ^ flip;
    };
    auto issue_c = [&](int kap, unsigned lds) {
        LAS char* sb = smem + lds + lw * 1024;
#pragma unroll
        for (int g = 0; g < 8; ++g) dma16((g & 1) ? ri : rr, sb + g * 2048, voA, kap * 128 + (g >> 1) * (16 * S_ * 2));
    };
    auto issue_x = [&](int kap, unsigned lds) {
        LAS char* sb = smem + lds + P1_AB + lw * X_ODD;
#pragma unroll
        for (int b = 0; b < 10; ++b)
            dma16(rx, sb + b * 1024, voB[b % 5], kap * (64 * D_ * 2) + (b / 5) * (32 * D_ * 2));
    };
    auto next_slot = [&](unsigned lds) { return lds == 2 * P1_SLOT ? 0u : lds + P1_SLOT; };
    issue_c(kap, lds);
    __builtin_amdgcn_sched_barrier(0);
#pragma unroll
    for (int b = 0; b < 5; ++b) {
        const unsigned c = b * 64 + lane, kr = __umul24(c, 3277) >> 16;
        voB[b] = __umul24(kr, D_ * 2 - 320) + __umul24(kr & 8, D_ * 2) + c * 16 + lw * (8 * D_ * 2);
    }
    issue_x(kap, lds);
    lds = next_slot(lds);
    issue_c(kap ^ 1, lds); issue_x(kap ^ 1, lds);
    lds = next_slot(lds);
    const u64 tag = ld_agent(ws + EPOCH_OFF + tb * 128) + 1;
    const char* pre = (const char*)wre + (hi * 4 + nj) * 80 * (D_ * 2);
    const char* pim = (const char*)wim + (hi * 4 + nj) * 80 * (D_ * 2);
    const unsigned voW = (row + (row & 12)) * (D_ * 2) + swz, voW4 = row * (D_ * 2) + swz;
    auto issue_w = [&](int u0, int u1) {
#pragma unroll 1
        for (int u = u0; u < u1; ++u) {
            int sg = u + 5 * gr;
            sg = sg >= 20 ? sg - 20 : sg;
            rsrc_t r = mk_rsrc(sg < 10 ? pre : pim, 80 * D_ * 2);
            const int ks = sg < 10 ? sg : sg - 10;
            LAS char* sb = smem + ((u + 11) & 15) * W_SLOT + lw * 1024;
#pragma unroll
            for (int j = 0; j < 5; ++j) dma16(r, sb + j * 2048, j < 4 ? voW : voW4, (32 * (j >> 1) + 4 * (j & 1)) * (D_ * 2) + ks * 128);
        }
    };
    if (!lng) { issue_w(0, 3); wait_vm<18 + 5 * 3>(); } else wait_vm<18>();
    const unsigned ride = lng ? 0x2330 : 0x110;
    int un = lng ? 0 : 3;
    kap ^= 2;
#pragma unroll 1
    for (int s = 0; s < n - 3; ++s) {
        wg_barrier();
        __builtin_amdgcn_sched_barrier(0);
        if (__builtin_expect((ride >> s) & 1, 0)) { issue_w(un, un + 1); ++un; }
        issue_c(kap, lds);
        __builtin_amdgcn_sched_barrier(0);
        int kn = kappa(s + 3);
        unsigned ln = next_slot(lds);
        asm volatile("" : "+s"(kn), "+s"(ln));
        __builtin_amdgcn_sched_barrier(0);
        issue_x(kap, lds);
        __builtin_amdgcn_sched_barrier(0);
        kap = kn; lds = ln;
        wait_vm<18>();
    }
    const char* pf = ws + FLAG_OFF + (tb * 8 + (hi ^ 1) * 4 + nj) * 128;
    const bool peek = lng && lw == 0 && lane == 0;
    u64 seen = 0, z = 0;
    wg_barrier();
    issue_c(kap, lds);
    if (peek) PEEK(seen, pf, z);
    issue_x(kap, lds);
    if (lng) {
        asm volatile("s_waitcnt vmcnt(10)" : "+v"(seen) :: "memory");
        if (peek && seen != tag) poll(pf, tag);
        wg_barrier();
        __builtin_amdgcn_s_sleep(4);
        issue_w(5, 8);
        wait_vm<15>(); wg_barrier();
        issue_w(8, 12);
        wg_barrier();
        issue_w(12, 15);
        wg_barrier();
    } else {
        wait_vm<18>(); wg_barrier();
        wait_vm<0>(); wg_barrier();
        wg_barrier();
        wg_barrier();
        __builtin_amdgcn_s_sleep(2);
        issue_w(5, 16);
    }
    __builtin_amdgcn_s_sleep(3);
    issue_w(lng ? 15 : 16, 20);
    wait_vm<0>(); wg_barrier();
}

struct XF { u32x2 lo, hi; };
struct P1Frags { u32x4 c[4]; XF x[5]; };
struct P1Addr { unsigned a0, a1, b0, b4; };
struct WF { u32x4 w[2][5]; };
__device__ __forceinline__ u32x4 cat(const XF& x) { return u32x4{x.lo[0], x.lo[1], x.hi[0], x.hi[1]}; }

template <int KS> __device__ __forceinline__ void p1_rd(int j, P1Frags& n, const P1Addr& p) {
    constexpr int KO = KS * 5120;
    const unsigned a = KS ? p.a1 : p.a0;
    if (j == 0) RDTR(n.x[0].lo, p.b0, KO);
    if (j == 1) RDTR(n.x[0].hi, p.b0, KO + 1280);
    if (j == 2) RD128(n.c[0], a, 0);
    if (j == 3) RD128(n.c[1], a, 2048);
    if (j == 4) RD128(n.c[2], a, 4096);
    if (j == 5) RD128(n.c[3], a, 6144);
    if (j == 6) RDTR(n.x[1].lo, p.b0, KO + 8);
    if (j == 7) RDTR(n.x[1].hi, p.b0, KO + 8 + 1280);
    if (j == 8) RDTR(n.x[2].lo, p.b0, KO + 64);
    if (j == 9) RDTR(n.x[2].hi, p.b0, KO + 64 + 1280);
    if (j == 10) RDTR(n.x[3].lo, p.b0, KO + 72);
    if (j == 11) RDTR(n.x[3].hi, p.b0, KO + 72 + 1280);
    if (j == 12) RDTR(n.x[4].lo, p.b4, KO);
    if (j == 13) RDTR(n.x[4].hi, p.b4, KO + 1280);
}
template <int KS, class H>
__device__ __forceinline__ void p1_half(f32x4 (&acc)[4][5], const P1Frags& f, P1Frags& n, const P1Addr& p, H hook) {
#pragma unroll
    for (int k = 0; k < 20; ++k) {
        u32x4 xx = cat(f.x[k >> 2]);
        MFMA(acc[k & 3][k >> 2], xx, f.c[k & 3]);
        p1_rd<KS>(k, n, p);
        hook(k);
    }
}
__device__ __forceinline__ void p1_move(int k, P1Addr& p, int d) {
    if (k == 14) { VADD(p.a0, d); VADD(p.b0, d); }
    if (k == 15) { VADD(p.a1, d); VADD(p.b4, d); }
}

__device__ __forceinline__ void compute(LAS char* smem, char* ws, char* y, int tb, int nj, int hi, int lng, int w, int lane) {
    const u64 epoch = ld_agent(ws + EPOCH_OFF + tb * 128);
    const unsigned base = (unsigned)(size_t)smem;
    const int wm = w >> 1, wn = w & 1;
    const int m = lane & 15, g4 = lane >> 4, q = (lane & 15) >> 2, p = lane & 3;
    const unsigned o0 = base + m * 128 + ((g4 ^ ((m >> 1) & 7)) << 4), o1 = o0 ^ 64;
    rsrc_t rpq = mk_rsrc(ws, PQ_BYTES);
    WF wf[4];
    unsigned q0 = o0 + 10 * W_SLOT, q1 = o1 + 10 * W_SLOT;
    {
        f32x4 acc[4][5];
#pragma unroll
        for (int g = 0; g < 4; ++g)
#pragma unroll
            for (int i = 0; i < 5; ++i) acc[g][i] = f32x4{0, 0, 0, 0};
        PIN5(acc[0]); PIN5(acc[1]); PIN5(acc[2]); PIN5(acc[3]);
        u32x4 id[3];
        {
            const unsigned one = (m & 1) ? 0x3f800000u : 0x3f80u;
#pragma unroll
            for (int r = 0; r < 4; ++r) {
                id[0][r] = g4 == q && r == (p >> 1) ? one : 0u;
                id[1][r] = g4 == q && r == 2 + (p >> 1) ? one : 0u;
                id[2][r] = 4 * g4 + r == (m >> 1) + 8 * wn ? one : 0u;
            }
            asm volatile("" : "+v"(id[0]), "+v"(id[1]), "+v"(id[2]));
        }
        const int n = lng ? NL : NS;
        unsigned so = lng ? (96 - NL) % 3 * P1_SLOT : (96 - NS) % 3 * P1_SLOT;
        const unsigned rowb = base + so + P1_AB + (g4 & 1) * X_ODD + ((g4 >> 1) * 8 + q) * 320;
        P1Addr pa = {o0 + so + wm * 8192, o1 + so + wm * 8192, rowb + wn * 192 + p * 16, rowb + 128 + wn * 32 + p * 8};
        P1Frags f0, f1;
        wg_barrier();
#pragma unroll
        for (int j = 0; j < 14; ++j) p1_rd<0>(j, f0, pa);
        int d = (so == 2 * P1_SLOT) ? -2 * P1_SLOT : P1_SLOT;
#pragma unroll 1
        for (int s = n - 2; s > 1; --s) {
            wait_lds<0>();
            p1_half<1>(acc, f0, f1, pa, [&](int k) { p1_move(k, pa, d); });
            wg_barrier();
            p1_half<0>(acc, f1, f0, pa, [&](int k) {
                if (k == 14) {
                    so += d;
                    d = (so == 2 * P1_SLOT) ? -2 * P1_SLOT : P1_SLOT;
                    asm volatile("" : "+s"(d), "+s"(so));
                }
            });
        }
        const unsigned sb = (4 * tb + 2 * wm) * TGS + nj * 5 * 1024;
        u32x4 pt[4][3];
        rsrc_t rpt = mk_rsrc(ws + PQ_HALF, lng ? PQ_HALF : 0);
        const unsigned v16 = wn * 3072 + lane * 16, vq = 2048 + lane * 16;
        unsigned sp[4];
#pragma unroll
        for (int o = 0; o < 4; ++o) sp[o] = sb + (o & 1) * TGS + (o >> 1) * 20 * 1024;
        auto ldp = [&](int k) {
            const int j = k / 3;
            if (k % 3 || j >= 12) return;
            if (j % 3 == 0) GLD(pt[j / 3][0], v16, rpt, sp[j / 3], 0);
            if (j % 3 == 1) GLD(pt[j / 3][1], v16, rpt, sp[j / 3], 1024);
            if (j % 3 == 2) GLD(pt[j / 3][2], vq, rpt, sp[j / 3], 0);
        };
        unsigned dst = sb + (lng ? 0 : PQ_HALF), vh = (g4 & 1) * TGS + 2048 + wn * 512 + (g4 >> 1) * 256 + m * 16;
        asm volatile("" : "+v"(vh));
        asm volatile("" : "+s"(dst));
        wait_lds<0>();
        p1_half<1>(acc, f0, f1, pa, [&](int k) { p1_move(k, pa, P1_SLOT); });
        wg_barrier();
        p1_half<0>(acc, f1, f0, pa, [&](int k) { ldp(k); });
        wait_lds<0>();
        p1_half<1>(acc, f0, f1, pa, [&](int k) { p1_move(k, pa, P1_SLOT); ldp(k + 20); });
        wg_barrier();
        p1_half<0>(acc, f1, f0, pa, [&](int k) { ldp(k + 40); });
        wait_lds<0>();
        p1_half<1>(acc, f0, f1, pa, [&](int) {});
        wait_lds<0>();
        __builtin_amdgcn_sched_barrier(0);
        auto rdw = [&](int u, int j) {
            if (j < 5) RD128(wf[u].w[0][j % 5], q0, (u + 1) * W_SLOT + (j % 5) * 2048);
            else RD128(wf[u].w[1][j % 5], q1, (u + 1) * W_SLOT + (j % 5) * 2048);
        };
        unsigned pk[8], ph[2][2];
        static_for<0, 48>([&](auto kc) {
            constexpr int k = kc, o = k / 10, g = (o & 1) * 2 + (o >> 1), i = k % 5;
            if constexpr (k < 40 && k % 10 == 5)
                asm volatile("s_waitcnt vmcnt(%3)" : "+v"(pt[o][0]), "+v"(pt[o][1]), "+v"(pt[o][2]) : "i"(k < 10 ? 9 : k < 20 ? 7 : 6) : "memory");
            if constexpr (k < 40) {
                const u32x4 a = (k % 10 < 5) ? cat(f1.x[i]) : id[i == 4 ? 2 : i & 1];
                const u32x4 b = (k % 10 < 5) ? f1.c[g] : pt[o][i >> 1];
                MFMA(acc[g][i], a, b);
            }
            if constexpr (k >= 20 && k < 23) {
                id[k - 20] ^= u32x4{0x80008000u, 0x80008000u, 0x80008000u, 0x80008000u};
                asm volatile("" : "+v"(id[k - 20]));
            }
            if constexpr (k >= 12 && (k - 12) % 10 < 6) {
                constexpr int e = (k - 12) / 10, h = (e & 1) * 2 + (e >> 1), j = (k - 12) % 10;
                if constexpr (j == 0)
                    asm volatile("s_nop 1" : "+v"(acc[h][0]), "+v"(acc[h][1]), "+v"(acc[h][2]), "+v"(acc[h][3]), "+v"(acc[h][4]));
                if constexpr (j < 5) {
                    unsigned& r0 = j < 4 ? pk[2 * (j & 3)] : ph[e & 1][0];
                    unsigned& r1 = j < 4 ? pk[2 * (j & 3) + 1] : ph[e & 1][1];
                    if constexpr (h & 1) { r0 = pack2n(acc[h][j][0], acc[h][j][1]); r1 = pack2n(acc[h][j][2], acc[h][j][3]); }
                    else { r0 = pack2(acc[h][j][0], acc[h][j][1]); r1 = pack2(acc[h][j][2], acc[h][j][3]); }
                }
                const unsigned so2 = dst + (e & 1) * TGS + (e >> 1) * 20 * 1024;
                if constexpr (j == 2) __builtin_amdgcn_raw_buffer_store_b128(u32x4{pk[0], pk[1], pk[2], pk[3]}, rpq, v16, so2, 16);
                if constexpr (j == 4) __builtin_amdgcn_raw_buffer_store_b128(u32x4{pk[4], pk[5], pk[6], pk[7]}, rpq, v16 + 1024, so2, 16);
                if constexpr (j == 5 && (e & 1)) {
                    swap16(ph[0][0], ph[1][0]); swap16(ph[0][1], ph[1][1]);
                    __builtin_amdgcn_raw_buffer_store_b128(u32x4{ph[0][0], ph[0][1], ph[1][0], ph[1][1]}, rpq, vh, dst + (e >> 1) * 20 * 1024, 16);
                }
                if constexpr (j == 2 || j >= 4) asm volatile("s_nop 1" ::: "memory");
            }
            if constexpr (k < 10) rdw(0, k);
            if constexpr (k >= 24 && k < 24 + 10) rdw(1, k - 24);
            __builtin_amdgcn_sched_barrier(0);
        });
        static_for<0, 20>([&](auto kc) { rdw(2 + kc / 10, kc % 10); if constexpr (kc == 9) wait_lds<4>(); });
        wg_barrier();
    }
    f32x4 acc[5];
#pragma unroll
    for (int j = 0; j < 5; ++j) acc[j] = f32x4{0, 0, 0, 0};
    PIN5(acc);
    const int tg = tb * 4 + w, et = hi * 4 + nj;
    const unsigned va = lane * 16;
    unsigned sab[4];
#pragma unroll
    for (int b = 0; b < 4; ++b) sab[b] = tg * TGS + (((tb >> 1) + b) & 3) * 10240;
    u64 tag = epoch + 1;
    char* mine = ws + FLAG_OFF + (tb * 8 + hi * 4) * 128 + nj * (lng ? 8 : 128);
    const char* theirs = ws + FLAG_OFF + (tb * 8 + ((tb & 1) ^ 1) * 4) * 128 + (lane & 3) * 8;
    asm volatile("" : "+s"(sab[0]), "+s"(sab[1]), "+s"(sab[2]), "+s"(sab[3]));
    asm volatile("" : "+v"(tag), "+v"(mine), "+v"(theirs));
    wait_vm<0>();
    wg_barrier();
    if (w == 1 && lane == 0) asm volatile("global_atomic_swap_x2 %0, %1, off" :: "v"(mine), "v"(tag) : "memory");
    if (w == 0 && lane < 4) poll(theirs, tag);
    wg_barrier();
    constexpr int AD = 6, A0 = 4;
    u32x4 a[2 * AD];
    auto lda = [&](int u) {
        const unsigned sa = sab[u / 5] + (u % 5) * 2048;
        a[2 * (u % AD)] = __builtin_amdgcn_raw_buffer_load_b128(rpq, va, sa, 16);
        a[2 * (u % AD) + 1] = __builtin_amdgcn_raw_buffer_load_b128(rpq, va + 1024, sa, 16);
    };
#pragma unroll
    for (int u = 0; u < A0; ++u) lda(u);
    if (et == 0 && w == 0 && lane == 0)
        __hip_atomic_store((GAS u64*)(ws + EPOCH_OFF + tb * 128), tag, __ATOMIC_RELAXED, __HIP_MEMORY_SCOPE_AGENT);
#pragma unroll
    for (int u = 0; u < 20; ++u) {
        if (u < 19) wait_lds<10>(); else wait_lds<0>();
        if (u == 1) { VADD(q0, -10 * W_SLOT); VADD(q1, -10 * W_SLOT); }
        if (u == 7 || u == 13) { VADD(q0, 6 * W_SLOT); VADD(q1, 6 * W_SLOT); }
#pragma unroll
        for (int i = 0; i < 2; ++i)
#pragma unroll
            for (int j = 0; j < 5; ++j) {
                MFMA(acc[j], wf[u & 3].w[i][j], a[2 * (u % AD) + i]);
                if (u < 16) RE128(wf[u & 3].w[i][j], (i ? q1 : q0), (u == 0 ? 5 : (u - 1) % 6) * W_SLOT + j * 2048);
            }
        const int h0 = A0 + 2 * u < u + AD ? A0 + 2 * u : u + AD, h1 = A0 + 2 * u + 2 < u + 1 + AD ? A0 + 2 * u + 2 : u + 1 + AD;
#pragma unroll
        for (int v = h0; v < h1; ++v) if (v < 20) lda(v);
    }
    asm volatile("s_nop 7\n\ts_nop 7" : "+v"(acc[0]), "+v"(acc[1]), "+v"(acc[2]), "+v"(acc[3]), "+v"(acc[4]));
    rsrc_t ry = mk_rsrc(y, S_ * D_ * 2);
    const unsigned vy = m * (D_ * 2) + g4 * 16, sy = tg * (16 * D_ * 2) + et * 160;
    __builtin_amdgcn_raw_buffer_store_b128(pack_pair(acc[0], acc[1]), ry, vy, sy, 0);
    __builtin_amdgcn_raw_buffer_store_b128(pack_pair(acc[2], acc[3]), ry, vy + 64, sy, 0);
    asm volatile("s_nop 1" ::: "memory");
    __builtin_amdgcn_raw_buffer_store_b64(u32x2{pack2(acc[4][0], acc[4][1]), pack2(acc[4][2], acc[4][3])}, ry,
                                          m * (D_ * 2) + 128 + g4 * 8, sy, 0);
}
#endif

__global__ __launch_bounds__(384, 2) void fnet_fused(const void* __restrict__ x, const void* __restrict__ cr,
                                                     const void* __restrict__ ci, const void* __restrict__ wre,
                                                     const void* __restrict__ wim, char* ws, char* __restrict__ y) {
#if defined(__HIP_DEVICE_COMPILE__)
    __shared__ __attribute__((aligned(1024))) char smem_[163840];
    LAS char* smem = (LAS char*)smem_;
    const int xcd = blockIdx.x & 7, idx = blockIdx.x >> 3;
    const int hi = xcd & 1, nj = idx & 3, tb = 8 * (xcd >> 1) + (idx >> 2), lng = hi ^ (tb & 1);
    const int lane = threadIdx.x & 63;
    const int wid = __builtin_amdgcn_readfirstlane(threadIdx.x >> 6);
    if (__builtin_expect(wid < 4, 0)) compute(smem, ws, y, tb, nj, hi, lng, wid, lane);
    else loader(smem, x, cr, ci, wre, wim, ws, tb, nj, hi, lng, wid - 4, lane);
#endif
}

extern "C" void kernel_launch(void* const* d_in, const int* in_sizes, int n_in, void* d_out, int out_size,
                              void* d_ws, size_t ws_size, hipStream_t stream) {
    (void)in_sizes; (void)n_in; (void)out_size; (void)ws_size;
    fnet_fused<<<256, 384, 0, stream>>>(d_in[0], d_in[1], d_in[2], d_in[3], d_in[4], (char*)d_ws, (char*)d_out);
}
```

```cpp
#include <hip/hip_runtime.h>
#include <hip/hip_bf16.h>
#include <type_traits>

typedef __attribute__((ext_vector_type(2))) __bf16 bf16x2;
typedef __attribute__((ext_vector_type(2))) float f32x2;
typedef __attribute__((ext_vector_type(4))) float f32x4;
typedef __attribute__((ext_vector_type(4))) unsigned u32x4;
typedef __attribute__((ext_vector_type(2))) unsigned u32x2;
typedef __attribute__((ext_vector_type(8))) unsigned u32x8;
typedef unsigned long long u64;
#define LAS __attribute__((address_space(3)))
#define GAS __attribute__((address_space(1)))

constexpr int S_ = 2048, D_ = 640;
constexpr int TGS = 40 * 1024 + 128;
constexpr int PQ_HALF = 128 * TGS, PQ_BYTES = 2 * PQ_HALF;
constexpr int FLAG_OFF = PQ_BYTES;
constexpr int EPOCH_OFF = FLAG_OFF + 256 * 128;
constexpr int NS = 13, NL = 32 - NS;
constexpr int P1_AB = 16384, X_ODD = 10248, P1_SLOT = 36880, W_SLOT = 10240;
constexpr int SPIN_CAP = 1 << 20;

#if defined(__HIP_DEVICE_COMPILE__)
typedef __amdgpu_buffer_rsrc_t rsrc_t;
template <int N> __device__ __forceinline__ void wait_vm() { asm volatile("s_waitcnt vmcnt(%0)" ::"i"(N) : "memory"); }
template <int N> __device__ __forceinline__ void wait_lds() { asm volatile("s_waitcnt lgkmcnt(%0)" ::"i"(N) : "memory"); }
__device__ __forceinline__ void wg_barrier() { asm volatile("s_waitcnt lgkmcnt(0)\n\ts_barrier" ::: "memory"); }
__device__ __forceinline__ rsrc_t mk_rsrc(const void* p, unsigned bytes) {
    return __builtin_amdgcn_make_buffer_rsrc((void*)p, 0, bytes, 0x00020000);
}
__device__ __forceinline__ void dma16(rsrc_t r, LAS char* dst, unsigned voff, unsigned soff) {
    __builtin_amdgcn_raw_ptr_buffer_load_lds(r, (LAS void*)dst, 16, voff, soff, 0, 0);
}
__device__ __forceinline__ unsigned pack2(float a, float b) {
    f32x2 v = {a, b};
    return __builtin_bit_cast(unsigned, __builtin_convertvector(v, bf16x2));
}
__device__ __forceinline__ unsigned pack2n(float a, float b) {
    unsigned r;
    asm("v_cvt_pk_bf16_f32 %0, -%1, -%2" : "=v"(r) : "v"(a), "v"(b));
    return r;
}
__device__ __forceinline__ void swap16(unsigned& a, unsigned& b) { auto r = __builtin_amdgcn_permlane16_swap(a, b, false, false); a = r[0]; b = r[1]; }
__device__ __forceinline__ u32x4 pack_pair(f32x4 a, f32x4 b) {
    return u32x4{pack2(a[0], a[1]), pack2(a[2], a[3]), pack2(b[0], b[1]), pack2(b[2], b[3])};
}
template <int I, int N, class F> __device__ __forceinline__ void static_for(F&& f) {
    if constexpr (I < N) { f(std::integral_constant<int, I>{}); static_for<I + 1, N>(f); }
}
#define PIN5(a) asm volatile("" : "+v"(a[0]), "+v"(a[1]), "+v"(a[2]), "+v"(a[3]), "+v"(a[4]))
#define MFMA(acc, a, b) asm volatile("v_mfma_f32_16x16x32_bf16 %0, %1, %2, %0" : "+v"(acc) : "v"(a), "v"(b))
#define RD128(d, ad, off) asm volatile("ds_read_b128 %0, %1 offset:%2" : "=v"(d) : "v"(ad), "i"(off))
#define RE128(d, ad, off) asm volatile("ds_read_b128 %0, %1 offset:%2" : "+v"(d) : "v"(ad), "i"(off))
#define RDTR(d, ad, off) asm volatile("ds_read_b64_tr_b16 %0, %1 offset:%2" : "=v"(d) : "v"(ad), "i"(off))
#define GLD(d, vo, rs, so, off) asm volatile("buffer_load_dwordx4 %0, %1, %2, %3 offen offset:%4 sc1" : "=v"(d) : "v"(vo), "s"(rs), "s"(so), "i"(off) : "memory")
#define VADD(r, d) asm volatile("v_add_u32 %0, %1, %0" : "+v"(r) : "s"(d))
#define PEEK(v, p, z) asm volatile("global_atomic_or_x2 %0, %1, %2, off sc0" : "+&v"(v) : "v"(p), "v"(z) : "memory")

__device__ __forceinline__ u64 ld_agent(const char* p) {
    return __hip_atomic_load((GAS u64*)p, __ATOMIC_RELAXED, __HIP_MEMORY_SCOPE_AGENT);
}
__device__ __forceinline__ void poll(const char* p, u64 tag) {
    u64 v = 0, z = 0;
#pragma unroll 1
    for (int spin = 0; spin < SPIN_CAP; ++spin) {
        PEEK(v, p, z);
        asm volatile("s_waitcnt vmcnt(0)" : "+v"(v) :: "memory");
        if (__all(v == tag)) break;
    }
}

__device__ __forceinline__ void ld_barrier() { asm volatile("s_barrier" ::: "memory"); }
__device__ __forceinline__ void loader(LAS char* smem, const void* x, const void* cr, const void* ci, const void* wre,
                                       const void* wim, char* ws, const char* y, int tb, int nj, int hi, int lng, int lw, int lane,
                                       unsigned swz, unsigned voA, int flip, int kap) {
    const int row = 8 * lw + (lane >> 3), gr = (tb >> 1) & 3, gj = gr ^ nj;
    unsigned voB[5], lds = lng ? (96 - NL) % 3 * P1_SLOT : (96 - NS) % 3 * P1_SLOT;
    asm volatile("" : "+s"(lds));
    rsrc_t rr = mk_rsrc((const char*)cr + (size_t)tb * (64 * S_ * 2), 64 * S_ * 2);
    rsrc_t ri = mk_rsrc((const char*)ci + (size_t)tb * (64 * S_ * 2), 64 * S_ * 2);
    rsrc_t rx = mk_rsrc((const char*)x + nj * 320, S_ * D_ * 2 - nj * 320);
    const int n = lng ? NL : NS, rot = (((gr + nj) & 3) * (n - 12)) >> 2;
    auto kappa = [&](int s) {
        const int a = s + rot, b = a < n ? a : a + 12 - n;
        return (s < 12 ? s ^ gj : b) ^ flip;
    };
    auto issue_c = [&](int kap, unsigned lds) {
        LAS char* sb = smem + lds + lw * 1024;
#pragma unroll
        for (int g = 0; g < 8; ++g) dma16((g & 1) ? ri : rr, sb + g * 2048, voA, kap * 128 + (g >> 1) * (16 * S_ * 2));
    };
    auto issue_x = [&](int kap, unsigned lds) {
        LAS char* sb = smem + lds + P1_AB + lw * X_ODD;
#pragma unroll
        for (int b = 0; b < 10; ++b)
            dma16(rx, sb + b * 1024, voB[b % 5], kap * (64 * D_ * 2) + (b / 5) * (32 * D_ * 2));
    };
    auto next_slot = [&](unsigned lds) { return lds == 2 * P1_SLOT ? 0u : lds + P1_SLOT; };
    issue_c(kap, lds);
    __builtin_amdgcn_sched_barrier(0);
#pragma unroll
    for (int b = 0; b < 5; ++b) {
        const unsigned c = b * 64 + lane, kr = __umul24(c, 3277) >> 16;
        voB[b] = __umul24(kr, D_ * 2 - 320) + __umul24(kr & 8, D_ * 2) + c * 16 + lw * (8 * D_ * 2);
    }
    issue_x(kap, lds);
    unsigned unused = 0;
    if (lw == 1)
        asm volatile("s_load_dword %0, %1, %2\n\ts_load_dword %0, %1, %3\n\ts_load_dword %0, %4, %5"
                     : "+&s"(unused) : "s"(ws), "s"((4 * tb + nj) * TGS + 40960), "s"(PQ_HALF + (4 * tb + nj) * TGS + 40960),
                       "s"(y), "s"((tb * 64 + nj * 16) * (D_ * 2) + hi * 640));
    lds = next_slot(lds);
    issue_c(kap ^ 1, lds); issue_x(kap ^ 1, lds);
    lds = next_slot(lds);
    const u64 tag = ld_agent(ws + EPOCH_OFF + tb * 128) + 1;
    const char* pre = (const char*)wre + (hi * 4 + nj) * 80 * (D_ * 2);
    const char* pim = (const char*)wim + (hi * 4 + nj) * 80 * (D_ * 2);
    const unsigned voW = (row + (row & 12)) * (D_ * 2) + swz, voW4 = row * (D_ * 2) + swz;
    auto issue_w = [&](int u0, int u1) {
#pragma unroll 1
        for (int u = u0; u < u1; ++u) {
            int sg = u + 5 * gr;
            sg = sg >= 20 ? sg - 20 : sg;
            rsrc_t r = mk_rsrc(sg < 10 ? pre : pim, 80 * D_ * 2);
            const int ks = sg < 10 ? sg : sg - 10;
            LAS char* sb = smem + ((u + 11) & 15) * W_SLOT + lw * 1024;
#pragma unroll
            for (int j = 0; j < 5; ++j) dma16(r, sb + j * 2048, j < 4 ? voW : voW4, (32 * (j >> 1) + 4 * (j & 1)) * (D_ * 2) + ks * 128);
        }
    };
    if (!lng) { issue_w(0, 3); wait_vm<18 + 5 * 3>(); } else wait_vm<18>();
    const unsigned ride = lng ? 0x2330 : 0x110;
    int un = lng ? 0 : 3;
    kap ^= 2;
#pragma unroll 1
    for (int s = 0; s < n - 3; ++s) {
        ld_barrier();
        __builtin_amdgcn_sched_barrier(0);
        if (__builtin_expect((ride >> s) & 1, 0)) { issue_w(un, un + 1); ++un; }
        issue_c(kap, lds);
        __builtin_amdgcn_sched_barrier(0);
        int kn = kappa(s + 3);
        unsigned ln = next_slot(lds);
        asm volatile("" : "+s"(kn), "+s"(ln));
        __builtin_amdgcn_sched_barrier(0);
        issue_x(kap, lds);
        __builtin_amdgcn_sched_barrier(0);
        kap = kn; lds = ln;
        wait_vm<18>();
    }
    const char* pf = ws + FLAG_OFF + (tb * 8 + (hi ^ 1) * 4 + nj) * 128;
    const bool peek = lng && lw == 0 && lane == 0;
    u64 seen = 0, z = 0;
    ld_barrier();
    issue_c(kap, lds);
    if (peek) PEEK(seen, pf, z);
    issue_x(kap, lds);
    if (lng) {
        asm volatile("s_waitcnt vmcnt(10)" : "+v"(seen) :: "memory");
        if (peek && seen != tag) poll(pf, tag);
        ld_barrier();
        __builtin_amdgcn_s_sleep(4);
        issue_w(5, 8);
        wait_vm<15>(); ld_barrier();
        issue_w(8, 12);
        ld_barrier();
        issue_w(12, 15);
        ld_barrier();
    } else {
        wait_vm<18>(); ld_barrier();
        wait_vm<0>(); ld_barrier();
        ld_barrier();
        ld_barrier();
        __builtin_amdgcn_s_sleep(2);
        issue_w(5, 16);
    }
    __builtin_amdgcn_s_sleep(3);
    issue_w(lng ? 15 : 16, 20);
    wait_vm<0>(); ld_barrier();
    asm volatile("s_waitcnt lgkmcnt(0)" :: "s"(unused));
}

struct XF { u32x2 lo, hi; };
struct P1Frags { u32x4 c[4]; XF x[5]; };
struct P1Addr { unsigned a0, a1, b0, b4; };
struct WF { u32x4 w[2][5]; };
__device__ __forceinline__ u32x4 cat(const XF& x) { return u32x4{x.lo[0], x.lo[1], x.hi[0], x.hi[1]}; }

template <int KS> __device__ __forceinline__ void p1_rd(int j, P1Frags& n, const P1Addr& p) {
    constexpr int KO = KS * 5120;
    const unsigned a = KS ? p.a1 : p.a0;
    if (j == 0) RDTR(n.x[0].lo, p.b0, KO);
    if (j == 1) RDTR(n.x[0].hi, p.b0, KO + 1280);
    if (j == 2) RD128(n.c[0], a, 0);
    if (j == 3) RD128(n.c[1], a, 2048);
    if (j == 4) RD128(n.c[2], a, 4096);
    if (j == 5) RD128(n.c[3], a, 6144);
    if (j == 6) RDTR(n.x[1].lo, p.b0, KO + 8);
    if (j == 7) RDTR(n.x[1].hi, p.b0, KO + 8 + 1280);
    if (j == 8) RDTR(n.x[2].lo, p.b0, KO + 64);
    if (j == 9) RDTR(n.x[2].hi, p.b0, KO + 64 + 1280);
    if (j == 10) RDTR(n.x[3].lo, p.b0, KO + 72);
    if (j == 11) RDTR(n.x[3].hi, p.b0, KO + 72 + 1280);
    if (j == 12) RDTR(n.x[4].lo, p.b4, KO);
    if (j == 13) RDTR(n.x[4].hi, p.b4, KO + 1280);
}
template <int KS, class H>
__device__ __forceinline__ void p1_half(f32x4 (&acc)[4][5], const P1Frags& f, P1Frags& n, const P1Addr& p, H hook) {
#pragma unroll
    for (int k = 0; k < 20; ++k) {
        u32x4 xx = cat(f.x[k >> 2]);
        MFMA(acc[k & 3][k >> 2], xx, f.c[k & 3]);
        p1_rd<KS>(k, n, p);
        hook(k);
    }
}
__device__ __forceinline__ void p1_move(int k, P1Addr& p, int d) {
    if (k == 14) { VADD(p.a0, d); VADD(p.b0, d); }
    if (k == 15) { VADD(p.a1, d); VADD(p.b4, d); }
}

__device__ __forceinline__ void compute(LAS char* smem, char* ws, char* y, int tb, int nj, int hi, int lng, int w, int lane) {
    const u64 epoch = ld_agent(ws + EPOCH_OFF + tb * 128);
    const unsigned base = (unsigned)(size_t)smem;
    const int wm = w >> 1, wn = w & 1;
    const int m = lane & 15, g4 = lane >> 4, q = (lane & 15) >> 2, p = lane & 3;
    const unsigned o0 = base + m * 128 + ((g4 ^ ((m >> 1) & 7)) << 4), o1 = o0 ^ 64;
    rsrc_t rpq = mk_rsrc(ws, PQ_BYTES);
    WF wf[4];
    unsigned q0 = o0 + 10 * W_SLOT, q1 = o1 + 10 * W_SLOT;
    {
        f32x4 acc[4][5];
#pragma unroll
        for (int g = 0; g < 4; ++g)
#pragma unroll
            for (int i = 0; i < 5; ++i) acc[g][i] = f32x4{0, 0, 0, 0};
        PIN5(acc[0]); PIN5(acc[1]); PIN5(acc[2]); PIN5(acc[3]);
        u32x4 id[3];
        {
            const unsigned one = (m & 1) ? 0x3f800000u : 0x3f80u;
#pragma unroll
            for (int r = 0; r < 4; ++r) {
                id[0][r] = g4 == q && r == (p >> 1) ? one : 0u;
                id[1][r] = g4 == q && r == 2 + (p >> 1) ? one : 0u;
                id[2][r] = 4 * g4 + r == (m >> 1) + 8 * wn ? one : 0u;
            }
            asm volatile("" : "+v"(id[0]), "+v"(id[1]), "+v"(id[2]));
        }
        const int n = lng ? NL : NS;
        unsigned so = lng ? (96 - NL) % 3 * P1_SLOT : (96 - NS) % 3 * P1_SLOT;
        const unsigned rowb = base + so + P1_AB + (g4 & 1) * X_ODD + ((g4 >> 1) * 8 + q) * 320;
        P1Addr pa = {o0 + so + wm * 8192, o1 + so + wm * 8192, rowb + wn * 192 + p * 16, rowb + 128 + wn * 32 + p * 8};
        P1Frags f0, f1;
        wg_barrier();
#pragma unroll
        for (int j = 0; j < 14; ++j) p1_rd<0>(j, f0, pa);
        int d = (so == 2 * P1_SLOT) ? -2 * P1_SLOT : P1_SLOT;
#pragma unroll 1
        for (int s = n - 2; s > 1; --s) {
            wait_lds<0>();
            p1_half<1>(acc, f0, f1, pa, [&](int k) { p1_move(k, pa, d); });
            wg_barrier();
            p1_half<0>(acc, f1, f0, pa, [&](int k) {
                if (k == 14) {
                    so += d;
                    d = (so == 2 * P1_SLOT) ? -2 * P1_SLOT : P1_SLOT;
                    asm volatile("" : "+s"(d), "+s"(so));
                }
            });
        }
        const unsigned sb = (4 * tb + 2 * wm) * TGS + nj * 5 * 1024;
        u32x4 pt[4][3];
        rsrc_t rpt = mk_rsrc(ws + PQ_HALF, lng ? PQ_HALF : 0);
        const unsigned v16 = wn * 3072 + lane * 16, vq = 2048 + lane * 16;
        unsigned sp[4];
#pragma unroll
        for (int o = 0; o < 4; ++o) sp[o] = sb + (o & 1) * TGS + (o >> 1) * 20 * 1024;
        auto ldp = [&](int k) {
            const int j = k / 3;
            if (k % 3 || j >= 12) return;
            if (j % 3 == 0) GLD(pt[j / 3][0], v16, rpt, sp[j / 3], 0);
            if (j % 3 == 1) GLD(pt[j / 3][1], v16, rpt, sp[j / 3], 1024);
            if (j % 3 == 2) GLD(pt[j / 3][2], vq, rpt, sp[j / 3], 0);
        };
        unsigned dst = sb + (lng ? 0 : PQ_HALF), vh = (g4 & 1) * TGS + 2048 + wn * 512 + (g4 >> 1) * 256 + m * 16;
        asm volatile("" : "+v"(vh));
        asm volatile("" : "+s"(dst));
        wait_lds<0>();
        p1_half<1>(acc, f0, f1, pa, [&](int k) { p1_move(k, pa, P1_SLOT); });
        wg_barrier();
        p1_half<0>(acc, f1, f0, pa, [&](int k) { ldp(k); });
        wait_lds<0>();
        p1_half<1>(acc, f0, f1, pa, [&](int k) { p1_move(k, pa, P1_SLOT); ldp(k + 20); });
        wg_barrier();
        p1_half<0>(acc, f1, f0, pa, [&](int k) { ldp(k + 40); });
        wait_lds<0>();
        p1_half<1>(acc, f0, f1, pa, [&](int) {});
        wait_lds<0>();
        __builtin_amdgcn_sched_barrier(0);
        auto rdw = [&](int u, int j) {
            if (j < 5) RD128(wf[u].w[0][j % 5], q0, (u + 1) * W_SLOT + (j % 5) * 2048);
            else RD128(wf[u].w[1][j % 5], q1, (u + 1) * W_SLOT + (j % 5) * 2048);
        };
        unsigned pk[8], ph[2][2];
        static_for<0, 48>([&](auto kc) {
            constexpr int k = kc, o = k / 10, g = (o & 1) * 2 + (o >> 1), i = k % 5;
            if constexpr (k < 40 && k % 10 == 5)
                asm volatile("s_waitcnt vmcnt(%3)" : "+v"(pt[o][0]), "+v"(pt[o][1]), "+v"(pt[o][2]) : "i"(k < 10 ? 9 : k < 20 ? 7 : 6) : "memory");
            if constexpr (k < 40) {
                const u32x4 a = (k % 10 < 5) ? cat(f1.x[i]) : id[i == 4 ? 2 : i & 1];
                const u32x4 b = (k % 10 < 5) ? f1.c[g] : pt[o][i >> 1];
                MFMA(acc[g][i], a, b);
            }
            if constexpr (k >= 20 && k < 23) {
                id[k - 20] ^= u32x4{0x80008000u, 0x80008000u, 0x80008000u, 0x80008000u};
                asm volatile("" : "+v"(id[k - 20]));
            }
            if constexpr (k >= 12 && (k - 12) % 10 < 6) {
                constexpr int e = (k - 12) / 10, h = (e & 1) * 2 + (e >> 1), j = (k - 12) % 10;
                if constexpr (j == 0)
                    asm volatile("s_nop 1" : "+v"(acc[h][0]), "+v"(acc[h][1]), "+v"(acc[h][2]), "+v"(acc[h][3]), "+v"(acc[h][4]));
                if constexpr (j < 5) {
                    unsigned& r0 = j < 4 ? pk[2 * (j & 3)] : ph[e & 1][0];
                    unsigned& r1 = j < 4 ? pk[2 * (j & 3) + 1] : ph[e & 1][1];
                    if constexpr (h & 1) { r0 = pack2n(acc[h][j][0], acc[h][j][1]); r1 = pack2n(acc[h][j][2], acc[h][j][3]); }
                    else { r0 = pack2(acc[h][j][0], acc[h][j][1]); r1 = pack2(acc[h][j][2], acc[h][j][3]); }
                }
                const unsigned so2 = dst + (e & 1) * TGS + (e >> 1) * 20 * 1024;
                if constexpr (j == 2) __builtin_amdgcn_raw_buffer_store_b128(u32x4{pk[0], pk[1], pk[2], pk[3]}, rpq, v16, so2, 16);
                if constexpr (j == 4) __builtin_amdgcn_raw_buffer_store_b128(u32x4{pk[4], pk[5], pk[6], pk[7]}, rpq, v16 + 1024, so2, 16);
                if constexpr (j == 5 && (e & 1)) {
                    swap16(ph[0][0], ph[1][0]); swap16(ph[0][1], ph[1][1]);
                    __builtin_amdgcn_raw_buffer_store_b128(u32x4{ph[0][0], ph[0][1], ph[1][0], ph[1][1]}, rpq, vh, dst + (e >> 1) * 20 * 1024, 16);
                }
                if constexpr (j == 2 || j >= 4) asm volatile("s_nop 1" ::: "memory");
            }
            if constexpr (k < 10) rdw(0, k);
            if constexpr (k >= 24 && k < 24 + 10) rdw(1, k - 24);
            __builtin_amdgcn_sched_barrier(0);
        });
        static_for<0, 20>([&](auto kc) { rdw(2 + kc / 10, kc % 10); if constexpr (kc == 9) wait_lds<4>(); });
        wg_barrier();
    }
    f32x4 acc[5];
#pragma unroll
    for (int j = 0; j < 5; ++j) acc[j] = f32x4{0, 0, 0, 0};
    PIN5(acc);
    const int tg = tb * 4 + w, et = hi * 4 + nj;
    const unsigned va = lane * 16;
    unsigned sab[4];
#pragma unroll
    for (int b = 0; b < 4; ++b) sab[b] = tg * TGS + (((tb >> 1) + b) & 3) * 10240;
    u64 tag = epoch + 1;
    char* mine = ws + FLAG_OFF + (tb * 8 + hi * 4) * 128 + nj * (lng ? 8 : 128);
    const char* theirs = ws + FLAG_OFF + (tb * 8 + ((tb & 1) ^ 1) * 4) * 128 + (lane & 3) * 8;
    asm volatile("" : "+s"(sab[0]), "+s"(sab[1]), "+s"(sab[2]), "+s"(sab[3]));
    asm volatile("" : "+v"(tag), "+v"(mine), "+v"(theirs));
    wait_vm<0>();
    wg_barrier();
    if (w == 1 && lane == 0) asm volatile("global_atomic_swap_x2 %0, %1, off" :: "v"(mine), "v"(tag) : "memory");
    if (w == 0 && lane < 4) poll(theirs, tag);
    wg_barrier();
    constexpr int AD = 6, A0 = 4;
    u32x4 a[2 * AD];
    auto lda = [&](int u) {
        const unsigned sa = sab[u / 5] + (u % 5) * 2048;
        a[2 * (u % AD)] = __builtin_amdgcn_raw_buffer_load_b128(rpq, va, sa, 16);
        a[2 * (u % AD) + 1] = __builtin_amdgcn_raw_buffer_load_b128(rpq, va + 1024, sa, 16);
    };
#pragma unroll
    for (int u = 0; u < A0; ++u) lda(u);
    if (et == 0 && w == 0 && lane == 0)
        __hip_atomic_store((GAS u64*)(ws + EPOCH_OFF + tb * 128), tag, __ATOMIC_RELAXED, __HIP_MEMORY_SCOPE_AGENT);
#pragma unroll
    for (int u = 0; u < 20; ++u) {
        if (u < 19) wait_lds<10>(); else wait_lds<0>();
        if (u == 1) { VADD(q0, -10 * W_SLOT); VADD(q1, -10 * W_SLOT); }
        if (u == 7 || u == 13) { VADD(q0, 6 * W_SLOT); VADD(q1, 6 * W_SLOT); }
#pragma unroll
        for (int i = 0; i < 2; ++i)
#pragma unroll
            for (int j = 0; j < 5; ++j) {
                MFMA(acc[j], wf[u & 3].w[i][j], a[2 * (u % AD) + i]);
                if (u < 16) RE128(wf[u & 3].w[i][j], (i ? q1 : q0), (u == 0 ? 5 : (u - 1) % 6) * W_SLOT + j * 2048);
            }
        const int h0 = A0 + 2 * u < u + AD ? A0 + 2 * u : u + AD, h1 = A0 + 2 * u + 2 < u + 1 + AD ? A0 + 2 * u + 2 : u + 1 + AD;
#pragma unroll
        for (int v = h0; v < h1; ++v) if (v < 20) lda(v);
    }
    asm volatile("s_nop 7\n\ts_nop 7" : "+v"(acc[0]), "+v"(acc[1]), "+v"(acc[2]), "+v"(acc[3]), "+v"(acc[4]));
    rsrc_t ry = mk_rsrc(y, S_ * D_ * 2);
    const unsigned vy = m * (D_ * 2) + g4 * 16, sy = tg * (16 * D_ * 2) + et * 160;
    __builtin_amdgcn_raw_buffer_store_b128(pack_pair(acc[0], acc[1]), ry, vy, sy, 0);
    __builtin_amdgcn_raw_buffer_store_b128(pack_pair(acc[2], acc[3]), ry, vy + 64, sy, 0);
    asm volatile("s_nop 1" ::: "memory");
    __builtin_amdgcn_raw_buffer_store_b64(u32x2{pack2(acc[4][0], acc[4][1]), pack2(acc[4][2], acc[4][3])}, ry,
                                          m * (D_ * 2) + 128 + g4 * 8, sy, 0);
}
#endif

__global__ __launch_bounds__(384, 2) void fnet_fused(const void* x_, const void* cr_, const void* ci_, const void* wre_,
                                                     const void* wim_, char* ws_, char* y_) {
#if defined(__HIP_DEVICE_COMPILE__)
    __shared__ __attribute__((aligned(1024))) char smem_[163840];
    LAS char* smem = (LAS char*)smem_;
    u32x8 ka; u32x4 kb; u32x2 kc;
    asm volatile("s_load_dwordx8 %0, %3, 0x0\n\ts_load_dwordx4 %1, %3, 0x20\n\ts_load_dwordx2 %2, %3, 0x30"
                 : "=&s"(ka), "=&s"(kb), "=&s"(kc) : "s"(__builtin_amdgcn_kernarg_segment_ptr()));
    __builtin_amdgcn_sched_barrier(0);
    const int xcd = blockIdx.x & 7, idx = blockIdx.x >> 3;
    int hi = xcd & 1, nj = idx & 3, tb = 8 * (xcd >> 1) + (idx >> 2), lng = hi ^ (tb & 1);
    const int lane = threadIdx.x & 63;
    int wid = __builtin_amdgcn_readfirstlane(threadIdx.x >> 6);
    const int row = 8 * (wid & 1) + (lane >> 3);
    unsigned swz = ((lane & 7) ^ ((row >> 1) & 7)) << 4, voA = row * (S_ * 2) + swz;
    int flip = (hi ? 31 : 0) ^ (tb & 8) * 2, kap = ((tb >> 1) & 3) ^ nj ^ flip;
    asm volatile("" : "+v"(swz), "+v"(voA));
    asm volatile("" : "+s"(hi), "+s"(nj), "+s"(tb), "+s"(lng), "+s"(wid), "+s"(flip), "+s"(kap));
    asm volatile("s_waitcnt lgkmcnt(0)" : "+s"(ka), "+s"(kb), "+s"(kc));
    auto ptr = [](unsigned lo, unsigned hi) { return (char*)(((u64)hi << 32) | lo); };
    char *ws = ptr(kb[2], kb[3]), *y = ptr(kc[0], kc[1]);
    if (__builtin_expect(wid < 4, 0)) compute(smem, ws, y, tb, nj, hi, lng, wid, lane);
    else loader(smem, ptr(ka[0], ka[1]), ptr(ka[2], ka[3]), ptr(ka[4], ka[5]), ptr(ka[6], ka[7]), ptr(kb[0], kb[1]), ws, y,
                tb, nj, hi, lng, wid - 4, lane, swz, voA, flip, kap);
#endif
}

extern "C" void kernel_launch(void* const* d_in, const int* in_sizes, int n_in, void* d_out, int out_size,
                              void* d_ws, size_t ws_size, hipStream_t stream) {
    (void)in_sizes; (void)n_in; (void)out_size; (void)ws_size;
    fnet_fused<<<256, 384, 0, stream>>>(d_in[0], d_in[1], d_in[2], d_in[3], d_in[4], (char*)d_ws, (char*)d_out);
}
```
